```python
import torch

def _softmax(x, axis=-1):
    return torch.softmax(x, dim=axis)
def _gelu(x):
    return torch.nn.functional.gelu(x, approximate="tanh")
def _silu(x):
    return torch.nn.functional.silu(x)
def _sigmoid(x):
    return torch.sigmoid(x)
def _layernorm(x, axis=-1, eps=1e-5):
    m = x.mean(dim=axis, keepdim=True)
    v = x.var(dim=axis, keepdim=True, unbiased=False)
    return (x - m) / torch.sqrt(v + eps)
def _scatter_add(tgt, idx, src, axis=0):
    return tgt.clone().index_add_(axis, idx, src)
def _segment_sum(src, seg, n):
    out = torch.zeros((n,) + src.shape[1:], dtype=src.dtype)
    return out.index_add_(0, seg, src)
def _pad(x, pw):
    flat = tuple(v for pair in reversed(pw) for v in pair)
    return torch.nn.functional.pad(x, flat)

kernel_name = 'recurrent_scan__grouped_kv_mqa__glu_gated_swiglu__prompt'

def setup_inputs():

    _w = (lambda a: (lambda t: t.to(torch.bfloat16) if t.is_floating_point() else t)(torch.as_tensor(a)))
    def _A_setup():
        B, S, D, H = 1, 2048, 576, 32
        Dh = D // H
        return {
            "x": _w((lambda *s: torch.randn(*s, dtype=torch.bfloat16) * 0.05)(B, S, D)),
            "wq": _w((lambda *s: torch.randn(*s, dtype=torch.bfloat16) * 0.2)(D, H * Dh)),
            "wk": _w((lambda *s: torch.randn(*s, dtype=torch.bfloat16) * 0.2)(D, Dh)),
            "wv": _w((lambda *s: torch.randn(*s, dtype=torch.bfloat16) * 0.2)(D, Dh)),
            "wo": _w((lambda *s: torch.randn(*s, dtype=torch.bfloat16) * 0.2)(H * Dh, D)),
        }
    _A_in = _A_setup()

    def _B_setup():
        B, S, D, F = 1, 2048, 576, 4608
        return {
            "x": _w((lambda *s: torch.randn(*s, dtype=torch.bfloat16) * 0.05)(B, S, D)),
            "w1": _w((lambda *s: torch.randn(*s, dtype=torch.bfloat16) * 0.1)(D, F) * (2.0 / D) ** 0.5),
            "w3": _w((lambda *s: torch.randn(*s, dtype=torch.bfloat16) * 0.1)(D, F) * (2.0 / D) ** 0.5),
            "w2": _w((lambda *s: torch.randn(*s, dtype=torch.bfloat16) * 0.1)(F, D) * (2.0 / F) ** 0.5),
        }
    _B_in = _B_setup()

    return {
        "A_x": _A_in["x"],
        "A_wq": _A_in["wq"],
        "A_wk": _A_in["wk"],
        "A_wv": _A_in["wv"],
        "A_wo": _A_in["wo"],
        "B_x": _B_in["x"],
        "B_w1": _B_in["w1"],
        "B_w3": _B_in["w3"],
        "B_w2": _B_in["w2"],
    }

def reference(A_x, A_wq, A_wk, A_wv, A_wo, B_x, B_w1, B_w3, B_w2):
    def _A_ref(x, wq, wk, wv, wo):
        B, S, D = x.shape
        H = 32
        Dh = D // H
        q = (lambda x, *d: x.permute(*d))((x @ wq).reshape(B, S, H, Dh), 0, 2, 1, 3)
        k = x @ wk
        v = x @ wv
        scores = torch.einsum("bhqd,bkd->bhqk", q, k) * (Dh ** -0.5)
        p = _softmax(scores, axis=-1)
        o = torch.einsum("bhqk,bkd->bhqd", p, v)
        return (lambda x, *d: x.permute(*d))(o, 0, 2, 1, 3).reshape(B, S, H * Dh) @ wo

    def _B_ref(x, w1, w3, w2):
        gate = torch.einsum("bsd,df->bsf", x, w1)
        up = torch.einsum("bsd,df->bsf", x, w3)
        h = _silu(gate) * up
        out = torch.einsum("bsf,fd->bsd", h, w2)
        return out

    x = A_x
    xn = _layernorm(x, axis=-1)
    s = _A_ref(xn, A_wq, A_wk, A_wv, A_wo)
    g = _silu(_B_ref(xn, B_w1, B_w3, B_w2))
    return x + s * g
```

```cpp
#include <hip/hip_runtime.h>
#include <hip/hip_bf16.h>
#include <stdint.h>

typedef unsigned short u16;
__device__ __forceinline__ float bf2f(u16 v){ return __uint_as_float(((unsigned)v)<<16); }
__device__ __forceinline__ u16 f2bf(float x){ unsigned u=__float_as_uint(x); u += 0x7fffu + ((u>>16)&1u); return (u16)(u>>16); }
__device__ __forceinline__ float rbf(float x){ return bf2f(f2bf(x)); }

constexpr int S=2048, D=576, H=32, DH=18, F=4608;

__global__ void ln_kernel(const u16* x, u16* xn){
  int row=blockIdx.x; int t=threadIdx.x;
  float v[9]; float s=0;
  for(int i=0;i<9;i++){ v[i]=bf2f(x[row*D+t+64*i]); s+=v[i]; }
  for(int o=32;o>0;o>>=1) s+=__shfl_xor(s,o);
  float mean=s/D;
  float q=0; for(int i=0;i<9;i++){ float d=v[i]-mean; q+=d*d; }
  for(int o=32;o>0;o>>=1) q+=__shfl_xor(q,o);
  float var=rbf(q/D);
  float m=rbf(mean);
  float den=rbf(sqrtf(rbf(var+1e-5f)));
  for(int i=0;i<9;i++){ xn[row*D+t+64*i]=f2bf(rbf(v[i]-m)/den); }
}

__global__ void gemm_naive(const u16* A,const u16* B,u16* C,int M,int N,int K){
  __shared__ float As[16][17], Bs[16][17];
  int tx=threadIdx.x, ty=threadIdx.y;
  int row=blockIdx.y*16+ty, col=blockIdx.x*16+tx;
  float acc=0;
  for(int k0=0;k0<K;k0+=16){
    As[ty][tx]=(row<M && k0+tx<K)? bf2f(A[(long)row*K+k0+tx]):0.f;
    Bs[ty][tx]=(col<N && k0+ty<K)? bf2f(B[(long)(k0+ty)*N+col]):0.f;
    __syncthreads();
    for(int k=0;k<16;k++) acc+=As[ty][k]*Bs[k][tx];
    __syncthreads();
  }
  if(row<M&&col<N) C[(long)row*N+col]=f2bf(acc);
}

__global__ void attn_naive(const u16* q,const u16* k,const u16* v,u16* o){
  int idx=blockIdx.x*blockDim.x+threadIdx.x;
  float qr[DH]; for(int d=0;d<DH;d++) qr[d]=bf2f(q[(long)idx*DH+d]);
  const float sc=0.23570226039551584f;
  float m=-INFINITY;
  for(int j=0;j<S;j++){ float s=0; for(int d=0;d<DH;d++) s+=qr[d]*bf2f(k[j*DH+d]); s=(s*sc); m=fmaxf(m,s);}
  float l=0;
  for(int j=0;j<S;j++){ float s=0; for(int d=0;d<DH;d++) s+=qr[d]*bf2f(k[j*DH+d]); s=(s*sc); l+=__expf(s-m);}
  float acc[DH]; for(int d=0;d<DH;d++) acc[d]=0;
  float il=1.f/l;
  for(int j=0;j<S;j++){ float s=0; for(int d=0;d<DH;d++) s+=qr[d]*bf2f(k[j*DH+d]); s=(s*sc); float p=(__expf(s-m)*il);
    for(int d=0;d<DH;d++) acc[d]+=p*bf2f(v[j*DH+d]); }
  for(int d=0;d<DH;d++) o[(long)idx*DH+d]=f2bf(acc[d]);
}

__global__ void swiglu_k(const u16* g,const u16* u,u16* h,int n){
  int i=blockIdx.x*blockDim.x+threadIdx.x; if(i>=n) return;
  float a=bf2f(g[i]); float s=rbf(a/(1.f+__expf(-a))); h[i]=f2bf(s*bf2f(u[i]));
}
__global__ void final_k(const u16* x,const u16* s,const u16* f,u16* out,int n){
  int i=blockIdx.x*blockDim.x+threadIdx.x; if(i>=n) return;
  float a=bf2f(f[i]); float g=rbf(a/(1.f+__expf(-a))); out[i]=f2bf(bf2f(x[i])+rbf(bf2f(s[i])*g));
}

extern "C" void kernel_launch(void* const* d_in, const int* in_sizes, int n_in,
                              void* d_out, int out_size, void* d_ws, size_t ws_size,
                              hipStream_t stream) {
  const u16* x=(const u16*)d_in[0]; const u16* wq=(const u16*)d_in[1]; const u16* wk=(const u16*)d_in[2];
  const u16* wv=(const u16*)d_in[3]; const u16* wo=(const u16*)d_in[4];
  const u16* w1=(const u16*)d_in[6]; const u16* w3=(const u16*)d_in[7]; const u16* w2=(const u16*)d_in[8];
  u16* ws=(u16*)d_ws;
  u16* xn=ws; u16* q=xn+S*D; u16* k=q+S*D; u16* v=k+S*32; u16* o=v+S*32; u16* s=o+S*D; u16* f=s+S*D;
  u16* g=f+S*D; u16* u=g+(long)S*F; u16* h=u+(long)S*F;
  ln_kernel<<<S,64,0,stream>>>(x,xn);
  dim3 b(16,16);
  gemm_naive<<<dim3(D/16,S/16),b,0,stream>>>(xn,wq,q,S,D,D);
  gemm_naive<<<dim3(2,S/16),b,0,stream>>>(xn,wk,k,S,DH,D);
  gemm_naive<<<dim3(2,S/16),b,0,stream>>>(xn,wv,v,S,DH,D);
  attn_naive<<<S*H/64,64,0,stream>>>(q,k,v,o);
  gemm_naive<<<dim3(D/16,S/16),b,0,stream>>>(o,wo,s,S,D,D);
  gemm_naive<<<dim3(F/16,S/16),b,0,stream>>>(xn,w1,g,S,F,D);
  gemm_naive<<<dim3(F/16,S/16),b,0,stream>>>(xn,w3,u,S,F,D);
  swiglu_k<<<S*F/256,256,0,stream>>>(g,u,h,S*F);
  gemm_naive<<<dim3(D/16,S/16),b,0,stream>>>(h,w2,f,S,D,F);
  final_k<<<S*D/256,256,0,stream>>>(x,s,f,(u16*)d_out,S*D);
}
```

```cpp
#include <hip/hip_runtime.h>
#include <stdint.h>

typedef unsigned short u16;
typedef unsigned int u32;
typedef __attribute__((ext_vector_type(8))) __bf16 bf16x8;
typedef __attribute__((ext_vector_type(2))) __bf16 bf16x2;
typedef __attribute__((ext_vector_type(4))) short s16x4;
typedef __attribute__((ext_vector_type(16))) float f32x16;
typedef __attribute__((ext_vector_type(4))) float f32x4;
typedef __attribute__((ext_vector_type(2))) float f32x2;
typedef __attribute__((ext_vector_type(4))) u32 u32x4;
typedef __attribute__((ext_vector_type(2))) u32 u32x2;
typedef u32x4 u32x4_a4 __attribute__((aligned(4)));
#define LDS_AS __attribute__((address_space(3)))
#define SCHED_FENCE() __builtin_amdgcn_sched_barrier(0)

constexpr size_t XF_OFF = 0;
constexpr size_t Q_OFF = XF_OFF + 2359296;
constexpr size_t K1F_OFF = Q_OFF + 2359296;
constexpr size_t K2_OFF = K1F_OFF + 65536;
constexpr size_t VF_OFF = K2_OFF + 8192;
constexpr size_t OF_OFF = VF_OFF + 131072;
constexpr size_t WOF_OFF = OF_OFF + 2359296;
constexpr size_t PART_OFF = WOF_OFF + 663552;

__device__ __forceinline__ u32 cvtpk(float a, float b) {
  f32x2 v = {a, b};
  bf16x2 r = __builtin_convertvector(v, bf16x2);
  return __builtin_bit_cast(u32, r);
}
__device__ __forceinline__ float dot2bf(u32 a, u32 b, float c) {
  return __builtin_amdgcn_fdot2_f32_bf16(__builtin_bit_cast(bf16x2, a), __builtin_bit_cast(bf16x2, b), c, false);
}
__device__ __forceinline__ float lo2f(u32 v) { return __uint_as_float(v << 16); }
__device__ __forceinline__ float hi2f(u32 v) { return __uint_as_float(v & 0xffff0000u); }
__device__ __forceinline__ float rbf(float x) { return lo2f(cvtpk(x, x)); }
__device__ __forceinline__ f32x16 mfma32(u32x4 a, u32x4 b, f32x16 c) {
  return __builtin_amdgcn_mfma_f32_32x32x16_bf16(__builtin_bit_cast(bf16x8, a), __builtin_bit_cast(bf16x8, b), c, 0, 0, 0);
}
__device__ __forceinline__ f32x4 mfma16(u32x4 a, u32x4 b, f32x4 c) {
  return __builtin_amdgcn_mfma_f32_16x16x32_bf16(__builtin_bit_cast(bf16x8, a), __builtin_bit_cast(bf16x8, b), c, 0, 0, 0);
}
__device__ __forceinline__ u32x4 tr2(u32 addr0, u32 addr1) {
  s16x4 a = __builtin_amdgcn_ds_read_tr16_b64_v4i16((LDS_AS s16x4*)(uintptr_t)addr0);
  s16x4 b = __builtin_amdgcn_ds_read_tr16_b64_v4i16((LDS_AS s16x4*)(uintptr_t)addr1);
  u32x2 x = __builtin_bit_cast(u32x2, a), y = __builtin_bit_cast(u32x2, b);
  return u32x4{x[0], x[1], y[0], y[1]};
}
__device__ __forceinline__ u32x4 lds_rd16(u32 addr) { return *(LDS_AS u32x4*)(uintptr_t)addr; }
__device__ __forceinline__ void lds_wr16(u32 addr, u32x4 v) { *(LDS_AS u32x4*)(uintptr_t)addr = v; }
template <int N>
__device__ __forceinline__ void waitvm() { asm volatile("s_waitcnt vmcnt(%0)" ::"n"(N) : "memory"); }
__device__ __forceinline__ void waitlgkm0() { asm volatile("s_waitcnt lgkmcnt(0)" ::: "memory"); }
__device__ __forceinline__ void rawbar() { asm volatile("s_barrier" ::: "memory"); }
__device__ __forceinline__ void glds(u32 m0val, u32 voff, const void* sbase) {
  asm volatile("s_mov_b32 m0, %0\n\ts_nop 0\n\tglobal_load_lds_dwordx4 %1, %2" ::"s"(m0val), "v"(voff), "s"(sbase) : "memory");
}
__device__ __forceinline__ float silu_f(float g) {
  return g * __builtin_amdgcn_rcpf(1.0f + __builtin_amdgcn_exp2f(g * -1.44269504088896f));
}

constexpr float QSCALE = 0.23570226039551584f * 1.4426950408889634f;

__global__ __launch_bounds__(256) void k1_kernel(const u16* __restrict__ x, const u16* __restrict__ wq,
                                                 const u16* __restrict__ wk, const u16* __restrict__ wv,
                                                 const u16* __restrict__ wo, char* __restrict__ ws) {
  extern __shared__ __attribute__((aligned(16))) char smem[];
  const u32 lds0 = (u32)(uintptr_t)(LDS_AS char*)smem;
  const int b = blockIdx.x, tgi = b & 31, cg = b >> 5;
  const int tid = threadIdx.x, w = __builtin_amdgcn_readfirstlane(tid >> 6), l = tid & 63;
  const int tok = l & 15, kg = l >> 4;
  const u16* xr = x + (size_t)(64 * tgi + 16 * w + tok) * 576 + 8 * kg;
  u32x4 xv[18];
#pragma unroll
  for (int ks = 0; ks < 18; ks++) xv[ks] = *(const u32x4*)(xr + 32 * ks);
  if (cg < 7) {
    const char* src = (const char*)wq + cg * 160;
#pragma unroll 1
    for (int i = w; i < 90; i += 4) {
      int n = i * 64 + l;
      int rp = (n * 6554) >> 16;
      int c = n - rp * 10;
      int r = rp ^ (((rp >> 3) & 1) << 2);
      glds(lds0 + i * 1024, (u32)(r * 1152 + c * 16), src);
    }
  } else {
#pragma unroll
    for (int i = 0; i < 3; i++) {
      int r = tid + 256 * i;
      if (r < 576) {
        u32x4 q0 = *(const u32x4*)(wq + r * 576 + 560), q1 = *(const u32x4*)(wq + r * 576 + 568);
        u32x4 k0 = *(const u32x4_a4*)(wk + r * 18), k1 = *(const u32x4_a4*)(wk + r * 18 + 8);
        u32 k2 = *(const u32*)(wk + r * 18 + 16);
        u32x4 v0 = *(const u32x4_a4*)(wv + r * 18), v1 = *(const u32x4_a4*)(wv + r * 18 + 8);
        u32 v2 = *(const u32*)(wv + r * 18 + 16);
        u32 a = lds0 + (r ^ (((r >> 3) & 1) << 2)) * 160;
        lds_wr16(a, q0); lds_wr16(a + 16, q1);
        lds_wr16(a + 32, k0); lds_wr16(a + 48, k1);
        lds_wr16(a + 64, v0); lds_wr16(a + 80, v1);
        lds_wr16(a + 96, u32x4{k2, v2, 0u, 0u}); lds_wr16(a + 112, u32x4{0u, 0u, 0u, 0u});
      }
    }
  }
  const int wid = b * 4 + w;
  u16 wg[8];
  if (wid < 648) {
    int d16 = wid / 18, ks = wid - d16 * 18;
    const u16* p = wo + (size_t)(32 * ks + 8 * kg) * 576 + 16 * d16 + tok;
#pragma unroll
    for (int j = 0; j < 8; j++) wg[j] = p[j * 576];
  }
  u32 ones_u = 0x3F803F80u;
  asm volatile("" : "+v"(ones_u));
  float s0 = 0, s1 = 0, s2 = 0, s3 = 0;
#pragma unroll
  for (int ks = 0; ks < 18; ks++) {
    s0 = dot2bf(xv[ks][0], ones_u, s0);
    s1 = dot2bf(xv[ks][1], ones_u, s1);
    s2 = dot2bf(xv[ks][2], ones_u, s2);
    s3 = dot2bf(xv[ks][3], ones_u, s3);
  }
  float sum = (s0 + s1) + (s2 + s3);
  sum += __shfl_xor(sum, 16);
  sum += __shfl_xor(sum, 32);
  const float mean = sum * (1.0f / 576.0f);
  const float mb = rbf(mean);
  s0 = s1 = s2 = s3 = 0;
#pragma unroll
  for (int ks = 0; ks < 18; ks++) {
#pragma unroll
    for (int i = 0; i < 4; i += 2) {
      float a0 = lo2f(xv[ks][i]) - mb, a1 = hi2f(xv[ks][i]) - mb;
      float a2 = lo2f(xv[ks][i + 1]) - mb, a3 = hi2f(xv[ks][i + 1]) - mb;
      s0 = fmaf(a0, a0, s0); s1 = fmaf(a1, a1, s1); s2 = fmaf(a2, a2, s2); s3 = fmaf(a3, a3, s3);
      xv[ks][i] = cvtpk(a0, a1);
      xv[ks][i + 1] = cvtpk(a2, a3);
    }
  }
  float sq = (s0 + s1) + (s2 + s3);
  sq += __shfl_xor(sq, 16);
  sq += __shfl_xor(sq, 32);
  const float dm = mean - mb;
  float var = rbf(fmaxf(sq * (1.0f / 576.0f) - dm * dm, 0.0f));
  const float den = rbf(sqrtf(rbf(var + 1e-5f)));
  const float rden = 1.0f / den;
#pragma unroll
  for (int ks = 0; ks < 18; ks++)
#pragma unroll
    for (int i = 0; i < 4; i++) xv[ks][i] = cvtpk(lo2f(xv[ks][i]) * rden, hi2f(xv[ks][i]) * rden);

  {
    char* xf = ws + XF_OFF + (size_t)tgi * 73728 + (size_t)((kg >> 1) * 2 + (w >> 1)) * 1024 +
               ((kg & 1) * 32 + (w & 1) * 16 + tok) * 16;
#pragma unroll
    for (int ks = 0; ks < 18; ks++)
      if ((ks & 7) == cg) *(u32x4*)(xf + ks * 4096) = xv[ks];
  }
  if (wid < 648) {
    u32x4 f = {(u32)wg[0] | ((u32)wg[1] << 16), (u32)wg[2] | ((u32)wg[3] << 16), (u32)wg[4] | ((u32)wg[5] << 16),
               (u32)wg[6] | ((u32)wg[7] << 16)};
    *(u32x4*)(ws + WOF_OFF + (size_t)wid * 1024 + l * 16) = f;
  }
  waitvm<0>();
  __syncthreads();

  const int q4 = (l & 15) >> 2, p4 = l & 3;
  const u32 a0 = lds0 + (8 * kg + 4 * (kg & 1) + q4) * 160 + 8 * p4;
  const u32 a1 = lds0 + (8 * kg + 4 * (1 - (kg & 1)) + q4) * 160 + 8 * p4;
  f32x4 acc[5];
#pragma unroll
  for (int t = 0; t < 5; t++) acc[t] = f32x4{0, 0, 0, 0};
  const int token = 64 * tgi + 16 * w + tok;
  u16* Q = (u16*)(ws + Q_OFF);
  if (cg < 7) {
#pragma unroll
    for (int ks = 0; ks < 18; ks++) {
#pragma unroll
      for (int t = 0; t < 5; t++) {
        u32x4 wf = tr2(a0 + ks * 5120 + t * 32, a1 + ks * 5120 + t * 32);
        acc[t] = mfma16(wf, xv[ks], acc[t]);
      }
    }
#pragma unroll
    for (int t = 0; t < 5; t++) {
      u32x2 o;
      o[0] = cvtpk(rbf(acc[t][0]) * QSCALE, rbf(acc[t][1]) * QSCALE);
      o[1] = cvtpk(rbf(acc[t][2]) * QSCALE, rbf(acc[t][3]) * QSCALE);
      *(u32x2*)(Q + (size_t)token * 576 + 80 * cg + 16 * t + 4 * kg) = o;
    }
  } else {
#pragma unroll
    for (int ks = 0; ks < 18; ks++) {
      u32x4 wf0 = tr2(a0 + ks * 5120, a1 + ks * 5120);
      u32x4 wf1 = tr2(a0 + ks * 5120 + 32, a1 + ks * 5120 + 32);
      u32x4 wf2 = tr2(a0 + ks * 5120 + 64, a1 + ks * 5120 + 64);
      u32x4 wf3 = tr2(a0 + ks * 5120 + 96, a1 + ks * 5120 + 96);
      acc[0] = mfma16(wf0, xv[ks], acc[0]);
      acc[1] = mfma16(wf1, xv[ks], acc[1]);
      acc[2] = mfma16(xv[ks], wf2, acc[2]);
      acc[3] = mfma16(wf3, xv[ks], acc[3]);
      acc[4] = mfma16(xv[ks], wf3, acc[4]);
    }
    u32x2 o;
    o[0] = cvtpk(rbf(acc[0][0]) * QSCALE, rbf(acc[0][1]) * QSCALE);
    o[1] = cvtpk(rbf(acc[0][2]) * QSCALE, rbf(acc[0][3]) * QSCALE);
    *(u32x2*)(Q + (size_t)token * 576 + 560 + 4 * kg) = o;
    const int t16 = 4 * tgi + w;
    o[0] = cvtpk(acc[1][0], acc[1][1]); o[1] = cvtpk(acc[1][2], acc[1][3]);
    *(u32x2*)(ws + K1F_OFF + (size_t)(t16 >> 1) * 1024 + ((kg >> 1) * 32 + 16 * (t16 & 1) + tok) * 16 + (kg & 1) * 8) = o;
    if (kg == 0) *(u32*)(ws + K2_OFF + (size_t)token * 4) = cvtpk(acc[3][0], acc[3][1]);
    o[0] = cvtpk(acc[2][0], acc[2][1]); o[1] = cvtpk(acc[2][2], acc[2][3]);
    char* vb = ws + VF_OFF + (size_t)t16 * 1024 + (kg & 1) * 512 + (kg >> 1) * 8;
    *(u32x2*)(vb + tok * 16) = o;
    if (tok >= 2 && tok <= 4) {
      o[0] = cvtpk(acc[4][0], acc[4][1]); o[1] = cvtpk(acc[4][2], acc[4][3]);
      if (tok == 4) { o[0] = 0x3F803F80u; o[1] = 0x3F803F80u; }
      *(u32x2*)(vb + (14 + tok) * 16) = o;
    }
  }
}

__device__ __forceinline__ float max16(const f32x16& s) {
  float a = fmaxf(fmaxf(s[0], s[1]), s[2]), b = fmaxf(fmaxf(s[3], s[4]), s[5]);
  float c = fmaxf(fmaxf(s[6], s[7]), s[8]), d = fmaxf(fmaxf(s[9], s[10]), s[11]);
  float e = fmaxf(fmaxf(s[12], s[13]), s[14]);
  return fmaxf(fmaxf(fmaxf(a, b), fmaxf(c, d)), fmaxf(e, s[15]));
}
template <int NB>
__global__ __launch_bounds__(256, 2) void attn_kernel(char* __restrict__ ws) {
  __shared__ __attribute__((aligned(16))) u32x4 xch[4 * 2 * 3 * 64];
  const int b = blockIdx.x;
  const int tid = threadIdx.x, w = __builtin_amdgcn_readfirstlane(tid >> 6), l = tid & 63;
  const int h = l >> 5, c31 = l & 31;
  const int pair = w >> 1, hf = w & 1;
  const int tok0 = 8 * b + 4 * pair;
  const u16* Q = (const u16*)(ws + Q_OFF);
  u32x4 Q1[4], Q2[4];
#pragma unroll
  for (int i = 0; i < 4; i++) {
    const u16* qp = Q + (size_t)(tok0 + i) * 576 + c31 * 18;
    Q1[i] = *(const u32x4_a4*)(qp + 8 * h);
    u32 q2 = *(const u32*)(qp + 16);
    Q2[i] = u32x4{h ? 0u : q2, 0u, 0u, 0u};
  }
  const char* k1p = ws + K1F_OFF + l * 16;
  const char* k2p = ws + K2_OFF + c31 * 4;
  const char* vp = ws + VF_OFF + (h * 32 + (c31 < 18 ? c31 : 18)) * 16;
  const f32x16 zero16 = {};
  {
    u32x4 k1 = *(const u32x4*)k1p;
    u32x4 a2 = u32x4{*(const u32*)k2p, 0x3F803F80u, 0u, 0u};
#pragma unroll
    for (int i = 0; i < 4; i++) {
      f32x16 s = mfma32(k1, Q1[i], zero16);
      s = mfma32(a2, Q2[i], s);
      float m = max16(s);
      m = fmaxf(m, __shfl_xor(m, 32));
      float mr = m + 80.0f;
      float hi = rbf(mr), lo = rbf(mr - hi);
      Q2[i][1] = h ? 0u : cvtpk(-hi, -lo);
    }
  }
  f32x16 O[4];
#pragma unroll
  for (int i = 0; i < 4; i++) O[i] = zero16;
  const int t0 = 32 * hf;
  u32x4 K1[NB], V0[NB], V1[NB];
  u32 K2[NB];
  auto loadK = [&](int bufi, int tile) {
    K1[bufi] = *(const u32x4*)(k1p + (size_t)tile * 1024);
    K2[bufi] = *(const u32*)(k2p + (size_t)tile * 128);
  };
  auto loadV = [&](int bufi, int tile) {
    V0[bufi] = *(const u32x4*)(vp + (size_t)tile * 2048);
    V1[bufi] = *(const u32x4*)(vp + (size_t)tile * 2048 + 1024);
  };
#pragma unroll
  for (int j = 0; j < NB; j++) loadK(j, t0 + j);
#pragma unroll
  for (int j = 0; j < NB - 1; j++) loadV(j, t0 + j);
  f32x16 Sc = mfma32(K1[0], Q1[0], zero16);
  Sc = mfma32(u32x4{K2[0], 0x3F803F80u, 0u, 0u}, Q2[0], Sc);
  u32x4 Pp = {0u, 0u, 0u, 0u};
  V1[NB - 1] = u32x4{0u, 0u, 0u, 0u};
#pragma unroll 1
  for (int n0 = 0; n0 < 32; n0 += NB) {
#pragma unroll
    for (int u = 0; u < NB; u++) {
      const int n = n0 + u;
#pragma unroll
      for (int i = 0; i < 4; i++) {
        const int ni = (i + 1) & 3;
        const int nb = (i == 3) ? (u + 1) % NB : u;
        const int pi = (i + 3) & 3;
        const int pb = (i == 0) ? (u + NB - 1) % NB : u;
        f32x16 Sn = mfma32(K1[nb], Q1[ni], zero16);
        float e0 = __builtin_amdgcn_exp2f(Sc[0]), e1 = __builtin_amdgcn_exp2f(Sc[1]);
        float e2 = __builtin_amdgcn_exp2f(Sc[2]), e3 = __builtin_amdgcn_exp2f(Sc[3]);
        u32x4 P0, P1;
        P0[0] = cvtpk(e0, e1); P0[1] = cvtpk(e2, e3);
        SCHED_FENCE();
        Sn = mfma32(u32x4{K2[nb], 0x3F803F80u, 0u, 0u}, Q2[ni], Sn);
        e0 = __builtin_amdgcn_exp2f(Sc[4]); e1 = __builtin_amdgcn_exp2f(Sc[5]);
        e2 = __builtin_amdgcn_exp2f(Sc[6]); e3 = __builtin_amdgcn_exp2f(Sc[7]);
        P0[2] = cvtpk(e0, e1); P0[3] = cvtpk(e2, e3);
        SCHED_FENCE();
        O[pi] = mfma32(V1[pb], Pp, O[pi]);
        if (i == 0) loadV((u + NB - 1) % NB, (t0 + n + NB - 1) & 63);
        if (i == 3) loadK(u, (t0 + n + NB) & 63);
        e0 = __builtin_amdgcn_exp2f(Sc[8]); e1 = __builtin_amdgcn_exp2f(Sc[9]);
        e2 = __builtin_amdgcn_exp2f(Sc[10]); e3 = __builtin_amdgcn_exp2f(Sc[11]);
        P1[0] = cvtpk(e0, e1); P1[1] = cvtpk(e2, e3);
        SCHED_FENCE();
        O[i] = mfma32(V0[u], P0, O[i]);
        e0 = __builtin_amdgcn_exp2f(Sc[12]); e1 = __builtin_amdgcn_exp2f(Sc[13]);
        e2 = __builtin_amdgcn_exp2f(Sc[14]); e3 = __builtin_amdgcn_exp2f(Sc[15]);
        P1[2] = cvtpk(e0, e1); P1[3] = cvtpk(e2, e3);
        SCHED_FENCE();
        Pp = P1;
        Sc = Sn;
      }
    }
  }
  O[3] = mfma32(V1[NB - 1], Pp, O[3]);
  f32x16 Ka, Kb, Sa, Sb;
  if (hf == 0) { Ka = O[0]; Kb = O[1]; Sa = O[2]; Sb = O[3]; }
  else { Ka = O[2]; Kb = O[3]; Sa = O[0]; Sb = O[1]; }
#pragma unroll
  for (int j = 0; j < 3; j++) {
    xch[((w * 2 + 0) * 3 + j) * 64 + l] = __builtin_bit_cast(u32x4, f32x4{Sa[4 * j], Sa[4 * j + 1], Sa[4 * j + 2], Sa[4 * j + 3]});
    xch[((w * 2 + 1) * 3 + j) * 64 + l] = __builtin_bit_cast(u32x4, f32x4{Sb[4 * j], Sb[4 * j + 1], Sb[4 * j + 2], Sb[4 * j + 3]});
  }
  __syncthreads();
  const int pw = w ^ 1;
#pragma unroll
  for (int j = 0; j < 3; j++) {
    f32x4 ra = __builtin_bit_cast(f32x4, xch[((pw * 2 + 0) * 3 + j) * 64 + l]);
    f32x4 rb = __builtin_bit_cast(f32x4, xch[((pw * 2 + 1) * 3 + j) * 64 + l]);
#pragma unroll
    for (int r = 0; r < 4; r++) { Ka[4 * j + r] += ra[r]; Kb[4 * j + r] += rb[r]; }
  }
  const int mytok = tok0 + 2 * hf;
  {
    float z = 0.f;
#pragma unroll
    for (int r = 0; r < 12; r++) { z = fmaf(Ka[r], 0.f, z); z = fmaf(Kb[r], 0.f, z); }
    bool bad = !(z == 0.f) || !(Ka[10] >= 8e-28f) || !(Kb[10] >= 8e-28f);
    if (__any(bad)) {
      u32x4 q1a, q2a, q1b, q2b;
      if (hf == 0) { q1a = Q1[0]; q2a = Q2[0]; q1b = Q1[1]; q2b = Q2[1]; }
      else { q1a = Q1[2]; q2a = Q2[2]; q1b = Q1[3]; q2b = Q2[3]; }
      q2a[1] = 0u; q2b[1] = 0u;
      float ma = -INFINITY, mb = -INFINITY;
#pragma unroll 1
      for (int t = 0; t < 64; t++) {
        u32x4 k1 = *(const u32x4*)(k1p + (size_t)t * 1024);
        u32x4 a2 = u32x4{*(const u32*)(k2p + (size_t)t * 128), 0x3F803F80u, 0u, 0u};
        f32x16 s = mfma32(k1, q1a, zero16); s = mfma32(a2, q2a, s);
        ma = fmaxf(ma, max16(s));
        s = mfma32(k1, q1b, zero16); s = mfma32(a2, q2b, s);
        mb = fmaxf(mb, max16(s));
      }
      ma = fmaxf(ma, __shfl_xor(ma, 32));
      mb = fmaxf(mb, __shfl_xor(mb, 32));
      Ka = zero16; Kb = zero16;
#pragma unroll 1
      for (int t = 0; t < 64; t++) {
        u32x4 k1 = *(const u32x4*)(k1p + (size_t)t * 1024);
        u32x4 a2 = u32x4{*(const u32*)(k2p + (size_t)t * 128), 0x3F803F80u, 0u, 0u};
        u32x4 v0 = *(const u32x4*)(vp + (size_t)t * 2048), v1 = *(const u32x4*)(vp + (size_t)t * 2048 + 1024);
        f32x16 s = mfma32(k1, q1a, zero16); s = mfma32(a2, q2a, s);
        u32x4 p0, p1;
#pragma unroll
        for (int r = 0; r < 4; r++) {
          p0[r] = cvtpk(__builtin_amdgcn_exp2f(s[2 * r] - ma), __builtin_amdgcn_exp2f(s[2 * r + 1] - ma));
          p1[r] = cvtpk(__builtin_amdgcn_exp2f(s[8 + 2 * r] - ma), __builtin_amdgcn_exp2f(s[9 + 2 * r] - ma));
        }
        Ka = mfma32(v0, p0, Ka); Ka = mfma32(v1, p1, Ka);
        s = mfma32(k1, q1b, zero16); s = mfma32(a2, q2b, s);
#pragma unroll
        for (int r = 0; r < 4; r++) {
          p0[r] = cvtpk(__builtin_amdgcn_exp2f(s[2 * r] - mb), __builtin_amdgcn_exp2f(s[2 * r + 1] - mb));
          p1[r] = cvtpk(__builtin_amdgcn_exp2f(s[8 + 2 * r] - mb), __builtin_amdgcn_exp2f(s[9 + 2 * r] - mb));
        }
        Kb = mfma32(v0, p0, Kb); Kb = mfma32(v1, p1, Kb);
      }
    }
  }
  {
    const float ia = 1.0f / Ka[10], ib = 1.0f / Kb[10];
    char* of = ws + OF_OFF + (size_t)(mytok >> 4) * 18432 + (mytok & 15) * 16;
#pragma unroll
    for (int m = 0; m < 5; m++) {
      const int dd = (m == 0 ? 0 : m == 1 ? 2 : m == 2 ? 8 : m == 3 ? 10 : 16) + 4 * h;
      const int k = 18 * c31 + dd;
      const u32 off = (u32)((k >> 5) * 1024 + ((k >> 3) & 3) * 256 + (k & 7) * 2);
      if (m < 4 || h == 0) {
        *(u32*)(of + off) = cvtpk(Ka[2 * m] * ia, Ka[2 * m + 1] * ia);
        *(u32*)(of + off + 16) = cvtpk(Kb[2 * m] * ib, Kb[2 * m + 1] * ib);
      }
    }
  }
}

constexpr int FFN_RING0 = 73728;
constexpr int FFN_STAGE = 5120;
constexpr int FFN_LDS = FFN_RING0 + 4 * 4 * FFN_STAGE;

struct FfnLd {
  const char* s0;
  const char* s1;
  const char* sx;
  u32 vA, vB, vX;
};

__device__ __forceinline__ void glds_first(u32 m0val, u32 voff, const char* sbase) {
  asm volatile("s_mov_b32 m0, %0\n\ts_nop 0\n\tglobal_load_lds_dwordx4 %1, %2 offset:-2048" ::"s"(m0val), "v"(voff), "s"(sbase) : "memory");
}
template <int OFF>
__device__ __forceinline__ void glds_next(u32 voff, const char* sbase) {
  if constexpr (OFF == -1024) asm volatile("global_load_lds_dwordx4 %0, %1 offset:-1024" ::"v"(voff), "s"(sbase) : "memory");
  else if constexpr (OFF == 0) asm volatile("global_load_lds_dwordx4 %0, %1" ::"v"(voff), "s"(sbase) : "memory");
  else if constexpr (OFF == 1024) asm volatile("global_load_lds_dwordx4 %0, %1 offset:1024" ::"v"(voff), "s"(sbase) : "memory");
  else asm volatile("global_load_lds_dwordx4 %0, %1 offset:2048" ::"v"(voff), "s"(sbase) : "memory");
}
__device__ __forceinline__ void ffn_issue_stage(u32 m0val, const FfnLd& ld) {
  glds_first(m0val, ld.vA, ld.s0);
  glds_next<-1024>(ld.vB, ld.s0);
  glds_next<0>(ld.vA, ld.s1);
  glds_next<1024>(ld.vB, ld.s1);
  glds_next<2048>(ld.vX, ld.sx);
}

template <int SLOT, int WAITN, bool DOLOAD>
__device__ __forceinline__ void ffn_step(f32x16 (&acc)[8], f32x16& accx, u32x4 (&A)[5], u32x4 (&Bc)[2], u32 aAddr0,
                                         u32 aAddr1, u32 xAddr0, u32 xAddr1, u32 bAddr0, u32 bAddr1, u32 ringm0,
                                         FfnLd& ld, u32 kstep) {
  constexpr int NS = ((SLOT + 1) & 3) * FFN_STAGE;
  waitvm<WAITN>();
  SCHED_FENCE();
  acc[0] = mfma32(A[0], Bc[0], acc[0]);
  u32x4 Bn0 = lds_rd16(bAddr0), Bn1 = lds_rd16(bAddr1);
  SCHED_FENCE();
  acc[1] = mfma32(A[0], Bc[1], acc[1]);
  A[0] = tr2(aAddr0 + NS, aAddr1 + NS);
  SCHED_FENCE();
  acc[2] = mfma32(A[1], Bc[0], acc[2]);
  SCHED_FENCE();
  acc[3] = mfma32(A[1], Bc[1], acc[3]);
  A[1] = tr2((aAddr0 ^ 64) + NS, (aAddr1 ^ 64) + NS);
  SCHED_FENCE();
  acc[4] = mfma32(A[2], Bc[0], acc[4]);
  if constexpr (DOLOAD) glds_first(ringm0 + SLOT * FFN_STAGE, ld.vA, ld.s0);
  SCHED_FENCE();
  acc[5] = mfma32(A[2], Bc[1], acc[5]);
  A[2] = tr2(aAddr0 + NS + 2048, aAddr1 + NS + 2048);
  if constexpr (DOLOAD) glds_next<-1024>(ld.vB, ld.s0);
  SCHED_FENCE();
  acc[6] = mfma32(A[3], Bc[0], acc[6]);
  SCHED_FENCE();
  acc[7] = mfma32(A[3], Bc[1], acc[7]);
  A[3] = tr2((aAddr0 ^ 64) + NS + 2048, (aAddr1 ^ 64) + NS + 2048);
  if constexpr (DOLOAD) glds_next<0>(ld.vA, ld.s1);
  SCHED_FENCE();
  accx = mfma32(A[4], Bc[0], accx);
  A[4] = tr2(xAddr0 + NS, xAddr1 + NS);
  if constexpr (DOLOAD) {
    glds_next<1024>(ld.vB, ld.s1);
    glds_next<2048>(ld.vX, ld.sx);
    ld.vA += kstep; ld.vB += kstep; ld.vX += kstep;
  }
  SCHED_FENCE();
  Bc[0] = Bn0; Bc[1] = Bn1;
}

__global__ __launch_bounds__(256, 2) void ffn_kernel(const u16* __restrict__ w1, const u16* __restrict__ w3,
                                                     const u16* __restrict__ w2, char* __restrict__ ws) {
  extern __shared__ __attribute__((aligned(16))) char smem[];
  const u32 lds0 = (u32)(uintptr_t)(LDS_AS char*)smem;
  const int b = blockIdx.x, g = b & 7, tg = b >> 3;
  const int tid = threadIdx.x, w = __builtin_amdgcn_readfirstlane(tid >> 6);
  int l = tid & 63;
  const int tl0 = w >> 1;
  const int rot = (tg * 4) % 36;
  const u32 ring = lds0 + FFN_RING0 + w * 4 * FFN_STAGE;
  const u32 ringm0 = ring + 2048;

  FfnLd ld;
  auto set_lane_offsets = [&](u32 strd, int kst) {
    int lo = l;
    asm volatile("" : "+v"(lo));
    const int r = lo >> 3, c = (lo & 7) ^ (((r >> 1) & 1) << 2);
    const u32 koff = (u32)kst * 16u * strd;
    ld.vA = r * strd + c * 16 + koff;
    ld.vB = ld.vA + 8 * strd - 1024;
    ld.vX = (lo >> 2) * strd + (lo & 3) * 16 + koff;
  };
  auto set_bases = [&](int pass) {
    if (pass < 2) {
      const size_t co = (size_t)g * 1152 + (size_t)(2 * w + pass) * 128;
      ld.s0 = (const char*)w1 + co + 2048;
      ld.s1 = (const char*)w3 + co;
      ld.sx = (const char*)(pass == 0 ? w1 : w3) + (size_t)g * 1152 + 1024 + (w & 1) * 64 - 2048;
    } else {
      const char* base = (const char*)w2 + (size_t)g * 576 * 1152;
      ld.s0 = base + w * 256 + 2048;
      ld.s1 = base + w * 256 + 128;
      ld.sx = base + 1024 + (w & 1) * 64 - 2048;
    }
  };
  set_bases(0);
  set_lane_offsets(9216, rot);
#pragma unroll
  for (int s = 0; s < 4; s++) {
    ffn_issue_stage(ringm0 + s * FFN_STAGE, ld);
    ld.vA += 16 * 9216; ld.vB += 16 * 9216; ld.vX += 16 * 9216;
  }
  {
    const char* xf = ws + XF_OFF + (size_t)tg * 73728;
#pragma unroll 1
    for (int i = 0; i < 18; i++) glds(lds0 + (w * 18 + i) * 1024, (u32)((w * 18 + i) * 1024 + l * 16), xf);
  }
  waitvm<0>();
  rawbar();

  f32x16 acc[8], accx;
  u32x4 GX[2];
  u32x4 A[5], Bc[2];
  u32x4 H0[4][2];
  const f32x16 zero16 = {};
#pragma unroll
  for (int t = 0; t < 4; t++) { H0[t][0] = u32x4{0, 0, 0, 0}; H0[t][1] = u32x4{0, 0, 0, 0}; }
  GX[0] = u32x4{0, 0, 0, 0}; GX[1] = u32x4{0, 0, 0, 0};

#pragma unroll 1
  for (int pass = 0; pass < 3; pass++) {
    u32 a0, a1, x0, x1, bL0, bL1;
    {
      int lo = l;
      asm volatile("" : "+v"(lo));
      const int h = lo >> 5, G1 = (lo >> 4) & 1, q4 = (lo & 15) >> 2, p4 = lo & 3;
      const u32 swz = ((q4 >> 1) & 1) * 64;
      const int hm = pass < 2 ? 8 : 4, tm = 12 - hm;
      a0 = ring + (hm * h + q4) * 128 + ((G1 * 32 + 8 * p4) ^ swz); a1 = a0 + tm * 128;
      x0 = ring + 4096 + (hm * h + q4) * 64 + G1 * 32 + 8 * p4; x1 = x0 + tm * 64;
      bL0 = lds0 + tl0 * 1024 + lo * 16; bL1 = lds0 + (1 - tl0) * 1024 + lo * 16;
    }
#pragma unroll
    for (int i = 0; i < 8; i++) acc[i] = zero16;
    accx = zero16;
    Bc[0] = lds_rd16(bL0 + rot * 2048);
    Bc[1] = lds_rd16(bL1 + rot * 2048);
    waitvm<15>();
    SCHED_FENCE();
    A[0] = tr2(a0, a1);
    SCHED_FENCE();
    A[1] = tr2(a0 ^ 64, a1 ^ 64);
    SCHED_FENCE();
    A[2] = tr2(a0 + 2048, a1 + 2048);
    SCHED_FENCE();
    A[3] = tr2((a0 ^ 64) + 2048, (a1 ^ 64) + 2048);
    SCHED_FENCE();
    A[4] = tr2(x0, x1);
    SCHED_FENCE();
    const u32 strd = pass < 2 ? 9216u : 1152u;
    u32 kstep = 16u * strd;
    int kb = rot;
#pragma unroll 1
    for (int j = 0; j < 9; j++) {
      int kn = kb + 4;
      if (kn == 36) kn = 0;
      if (j == 8) {
        if (pass < 2) {
          const u32 st2 = pass == 0 ? 9216u : 1152u;
          set_bases(pass + 1); set_lane_offsets(st2, rot); kstep = 16u * st2;
        } else {
          ld.vA = 0; ld.vB = 0; ld.vX = 2048; kstep = 0;
        }
      } else if (kn == 0) {
        set_lane_offsets(strd, 0);
      }
      const u32 bb0 = bL0 + kb * 2048, bb1 = bL1 + kb * 2048;
      ffn_step<0, 10, true>(acc, accx, A, Bc, a0, a1, x0, x1, bb0 + 2048, bb1 + 2048, ringm0, ld, kstep);
      ffn_step<1, 10, true>(acc, accx, A, Bc, a0, a1, x0, x1, bb0 + 4096, bb1 + 4096, ringm0, ld, kstep);
      ffn_step<2, 10, true>(acc, accx, A, Bc, a0, a1, x0, x1, bb0 + 6144, bb1 + 6144, ringm0, ld, kstep);
      ffn_step<3, 10, true>(acc, accx, A, Bc, a0, a1, x0, x1, bL0 + kn * 2048, bL1 + kn * 2048, ringm0, ld, kstep);
      kb = kn;
    }
    if (pass == 0) {
#pragma unroll
      for (int t = 0; t < 4; t++)
#pragma unroll
        for (int s = 0; s < 2; s++) {
#pragma unroll
          for (int r = 0; r < 4; r++) {
            const int e = 8 * s + 2 * r;
            H0[t][s][r] = cvtpk(silu_f(acc[t][e]) * acc[4 + t][e], silu_f(acc[t][e + 1]) * acc[4 + t][e + 1]);
          }
          SCHED_FENCE();
        }
#pragma unroll
      for (int e = 0; e < 8; e++) GX[e >> 2][e & 3] = cvtpk(silu_f(accx[2 * e]), silu_f(accx[2 * e + 1]));
    } else if (pass == 1) {
      waitlgkm0();
      rawbar();
#pragma unroll
      for (int t = 0; t < 4; t++)
#pragma unroll
        for (int s = 0; s < 2; s++) {
          const int ft = t >> 1, ltt = t & 1;
          lds_wr16((ltt == 0 ? bL0 : bL1) + (8 * w + 2 * ft + s) * 2048, H0[t][s]);
        }
      SCHED_FENCE();
#pragma unroll
      for (int t = 0; t < 4; t++)
#pragma unroll
        for (int s = 0; s < 2; s++) {
          u32x4 hh;
#pragma unroll
          for (int r = 0; r < 4; r++) {
            const int e = 8 * s + 2 * r;
            hh[r] = cvtpk(silu_f(acc[t][e]) * acc[4 + t][e], silu_f(acc[t][e + 1]) * acc[4 + t][e + 1]);
          }
          const int ft = t >> 1, ltt = t & 1;
          lds_wr16((ltt == 0 ? bL0 : bL1) + (8 * w + 4 + 2 * ft + s) * 2048, hh);
          SCHED_FENCE();
        }
#pragma unroll
      for (int s = 0; s < 2; s++) {
        u32x4 hx;
#pragma unroll
        for (int r = 0; r < 4; r++) {
          const int e = 8 * s + 2 * r;
          hx[r] = cvtpk(lo2f(GX[s][r]) * accx[e], hi2f(GX[s][r]) * accx[e + 1]);
        }
        lds_wr16(bL0 + (32 + 2 * (w & 1) + s) * 2048, hx);
      }
      waitlgkm0();
      rawbar();
    } else {
      int lo = l;
      asm volatile("" : "+v"(lo));
      char* pb = ws + PART_OFF + (size_t)(g * 32 + tg) * 73728 + lo * 16;
#pragma unroll
      for (int t = 0; t < 8; t++) {
        const int dt = 4 * w + (t >> 1);
        const int tt = (t & 1) == 0 ? tl0 : 1 - tl0;
#pragma unroll
        for (int hh = 0; hh < 2; hh++) {
          u32x4 o;
#pragma unroll
          for (int r = 0; r < 4; r++) o[r] = cvtpk(acc[t][8 * hh + 2 * r], acc[t][8 * hh + 2 * r + 1]);
          *(u32x4*)(pb + (size_t)((dt * 2 + tt) * 2 + hh) * 1024) = o;
        }
      }
#pragma unroll
      for (int hh = 0; hh < 2; hh++) {
        u32x4 o;
#pragma unroll
        for (int r = 0; r < 4; r++) o[r] = cvtpk(accx[8 * hh + 2 * r], accx[8 * hh + 2 * r + 1]);
        *(u32x4*)(pb + (size_t)(((16 + (w & 1)) * 2 + tl0) * 2 + hh) * 1024) = o;
      }
      waitvm<0>();
    }
  }
}

__global__ __launch_bounds__(576) void kb_kernel(const u16* __restrict__ x, const char* __restrict__ ws,
                                                 u16* __restrict__ out) {
  __shared__ __attribute__((aligned(16))) char smem[36864];
  const u32 lds0 = (u32)(uintptr_t)(LDS_AS char*)smem;
  const int b = blockIdx.x, cg = b & 3, T = b >> 2;
  const int tid = threadIdx.x, w = __builtin_amdgcn_readfirstlane(tid >> 6), l = tid & 63;
  const int tok = l & 15, rg = l >> 4;
  const int d16 = 9 * cg + w;
  {
    const char* of = ws + OF_OFF + (size_t)(2 * T) * 18432;
#pragma unroll
    for (int i = 0; i < 4; i++) glds(lds0 + (w * 4 + i) * 1024, (u32)((w * 4 + i) * 1024 + l * 16), of);
  }
  u32x4 WA[18];
  {
    const char* wf = ws + WOF_OFF + (size_t)d16 * 18432 + l * 16;
#pragma unroll
    for (int ks = 0; ks < 18; ks++) WA[ks] = *(const u32x4*)(wf + ks * 1024);
  }
  u32x2 PT[2][8], XV[2];
  {
    const int tg = T >> 1, tt = T & 1;
#pragma unroll
    for (int t = 0; t < 2; t++) {
      const char* pp = ws + PART_OFF + (size_t)tg * 73728 + (size_t)(((d16 >> 1) * 2 + tt) * 2 + (d16 & 1)) * 1024 +
                       ((rg & 1) * 32 + 16 * t + tok) * 16 + (rg >> 1) * 8;
#pragma unroll
      for (int g = 0; g < 8; g++) PT[t][g] = *(const u32x2*)(pp + (size_t)g * 32 * 73728);
      XV[t] = *(const u32x2*)(x + (size_t)(32 * T + 16 * t + tok) * 576 + 16 * d16 + 4 * rg);
    }
  }
  waitvm<0>();
  __syncthreads();
  f32x4 acc[2] = {f32x4{0, 0, 0, 0}, f32x4{0, 0, 0, 0}};
#pragma unroll
  for (int ks = 0; ks < 18; ks++) {
#pragma unroll
    for (int t = 0; t < 2; t++) acc[t] = mfma16(WA[ks], lds_rd16(lds0 + (t * 18 + ks) * 1024 + l * 16), acc[t]);
  }
#pragma unroll
  for (int t = 0; t < 2; t++) {
    float f[4] = {0, 0, 0, 0};
#pragma unroll
    for (int g = 0; g < 8; g++) {
      f[0] += lo2f(PT[t][g][0]); f[1] += hi2f(PT[t][g][0]); f[2] += lo2f(PT[t][g][1]); f[3] += hi2f(PT[t][g][1]);
    }
    float xr[4] = {lo2f(XV[t][0]), hi2f(XV[t][0]), lo2f(XV[t][1]), hi2f(XV[t][1])};
    float o[4];
#pragma unroll
    for (int r = 0; r < 4; r++) {
      float fr = rbf(f[r]);
      float gg = rbf(silu_f(fr));
      float s = rbf(acc[t][r]);
      o[r] = xr[r] + rbf(s * gg);
    }
    *(u32x2*)(out + (size_t)(32 * T + 16 * t + tok) * 576 + 16 * d16 + 4 * rg) = u32x2{cvtpk(o[0], o[1]), cvtpk(o[2], o[3])};
  }
}

extern "C" void kernel_launch(void* const* d_in, const int* in_sizes, int n_in, void* d_out, int out_size, void* d_ws,
                              size_t ws_size, hipStream_t stream) {
  const u16* x = (const u16*)d_in[0];
  const u16* wq = (const u16*)d_in[1];
  const u16* wk = (const u16*)d_in[2];
  const u16* wv = (const u16*)d_in[3];
  const u16* wo = (const u16*)d_in[4];
  const u16* w1 = (const u16*)d_in[6];
  const u16* w3 = (const u16*)d_in[7];
  const u16* w2 = (const u16*)d_in[8];
  char* ws = (char*)d_ws;
  (void)hipFuncSetAttribute((const void*)k1_kernel, hipFuncAttributeMaxDynamicSharedMemorySize, 92160);
  (void)hipFuncSetAttribute((const void*)ffn_kernel, hipFuncAttributeMaxDynamicSharedMemorySize, FFN_LDS);
  k1_kernel<<<256, 256, 92160, stream>>>(x, wq, wk, wv, wo, ws);
  attn_kernel<4><<<256, 256, 0, stream>>>(ws);
  ffn_kernel<<<256, 256, FFN_LDS, stream>>>(w1, w3, w2, ws);
  kb_kernel<<<256, 576, 0, stream>>>(x, ws, (u16*)d_out);
}
```

```cpp
#include <hip/hip_runtime.h>
#include <stdint.h>

typedef unsigned short u16;
typedef unsigned int u32;
typedef __attribute__((ext_vector_type(8))) __bf16 bf16x8;
typedef __attribute__((ext_vector_type(2))) __bf16 bf16x2;
typedef __attribute__((ext_vector_type(4))) short s16x4;
typedef __attribute__((ext_vector_type(16))) float f32x16;
typedef __attribute__((ext_vector_type(4))) float f32x4;
typedef __attribute__((ext_vector_type(2))) float f32x2;
typedef __attribute__((ext_vector_type(4))) u32 u32x4;
typedef __attribute__((ext_vector_type(2))) u32 u32x2;
typedef __amdgpu_buffer_rsrc_t rsrc_t;
#define LDS_AS __attribute__((address_space(3)))
#define SCHED_FENCE() __builtin_amdgcn_sched_barrier(0)

constexpr u32 EPOCH_OFF = 0;
constexpr int KVREP = 16;
constexpr u32 KVF_OFF = 4096;
constexpr u32 PFL_OFF = KVF_OFF + KVREP * 1024;
constexpr u32 OFL_OFF = PFL_OFF + 4096;
constexpr u32 K1F_OFF = OFL_OFF + 4096;
constexpr u32 K2_OFF = K1F_OFF + 65536;
constexpr u32 VF_OFF = K2_OFF + 8192;
constexpr u32 OF_OFF = VF_OFF + 131072;
constexpr u32 PART_OFF = OF_OFF + 2359296;
constexpr u32 WS_END = PART_OFF + 18874368;

constexpr u32 RING0 = 73728;
constexpr u32 STG = 5120;
constexpr u32 SLOT = 4 * STG;
constexpr u32 QSLOT = RING0 + 3 * SLOT;
constexpr u32 KVRED = 156672;
constexpr u32 FLAGS = 162816;
constexpr u32 LDS_TOTAL = 163840;
constexpr u32 XCH0 = 110592;
constexpr u32 WOX0 = 73728;
constexpr u32 RED0 = 92160;
constexpr u32 GG0 = 153600;
constexpr u32 F_BAR = 0, F_KVP = 16, F_QRD = 32, F_QCN = 48, F_KVD = 64, F_PX = 80, F_OCNT = 96, F_PCNT = 100, F_PRD = 104;
constexpr u32 PH_DONE = 4;

__device__ __forceinline__ u32 cvtpk(float a, float b) {
  f32x2 v = {a, b};
  bf16x2 r = __builtin_convertvector(v, bf16x2);
  return __builtin_bit_cast(u32, r);
}
__device__ __forceinline__ float dot2bf(u32 a, u32 b, float c) {
  return __builtin_amdgcn_fdot2_f32_bf16(__builtin_bit_cast(bf16x2, a), __builtin_bit_cast(bf16x2, b), c, false);
}
__device__ __forceinline__ float lo2f(u32 v) { return __uint_as_float(v << 16); }
__device__ __forceinline__ float hi2f(u32 v) { return __uint_as_float(v & 0xffff0000u); }
__device__ __forceinline__ float rbf(float x) { return lo2f(cvtpk(x, x)); }
__device__ __forceinline__ f32x16 mfma32(u32x4 a, u32x4 b, f32x16 c) {
  return __builtin_amdgcn_mfma_f32_32x32x16_bf16(__builtin_bit_cast(bf16x8, a), __builtin_bit_cast(bf16x8, b), c, 0, 0, 0);
}
__device__ __forceinline__ f32x4 mfma16k16(u32x2 a, u32x2 b, f32x4 c) {
  return __builtin_amdgcn_mfma_f32_16x16x16bf16_1k(__builtin_bit_cast(s16x4, a), __builtin_bit_cast(s16x4, b), c, 0, 0, 0);
}
__device__ __forceinline__ u32x4 tr2(u32 addr0, u32 addr1) {
  s16x4 a = __builtin_amdgcn_ds_read_tr16_b64_v4i16((LDS_AS s16x4*)(uintptr_t)addr0);
  s16x4 b = __builtin_amdgcn_ds_read_tr16_b64_v4i16((LDS_AS s16x4*)(uintptr_t)addr1);
  u32x2 x = __builtin_bit_cast(u32x2, a), y = __builtin_bit_cast(u32x2, b);
  return u32x4{x[0], x[1], y[0], y[1]};
}
__device__ __forceinline__ u32x4 lds_rd16(u32 addr) { return *(LDS_AS u32x4*)(uintptr_t)addr; }
__device__ __forceinline__ u32x2 lds_rd8(u32 addr) { return *(LDS_AS u32x2*)(uintptr_t)addr; }
__device__ __forceinline__ u32 lds_rd4(u32 addr) { return *(LDS_AS u32*)(uintptr_t)addr; }
__device__ __forceinline__ void lds_wr16(u32 addr, u32x4 v) { *(LDS_AS u32x4*)(uintptr_t)addr = v; }
__device__ __forceinline__ void lds_wr4(u32 addr, u32 v) { *(LDS_AS u32*)(uintptr_t)addr = v; }
template <int N>
__device__ __forceinline__ void waitvm() { asm volatile("s_waitcnt vmcnt(%0)" ::"n"(N) : "memory"); }
__device__ __forceinline__ void waitlgkm0() { asm volatile("s_waitcnt lgkmcnt(0)" ::: "memory"); }
__device__ __forceinline__ void rawbar() { asm volatile("s_waitcnt lgkmcnt(0)\n\ts_barrier" ::: "memory"); }
__device__ __forceinline__ void glds(u32 m0val, u32 voff, const void* sbase) {
  asm volatile("s_mov_b32 m0, %0\n\ts_nop 0\n\tglobal_load_lds_dwordx4 %1, %2" ::"s"(m0val), "v"(voff), "s"(sbase) : "memory");
}
__device__ __forceinline__ float silu_f(float g) {
  return g * __builtin_amdgcn_rcpf(1.0f + __builtin_amdgcn_exp2f(g * -1.44269504088896f));
}
__device__ __forceinline__ u32x4 ld16c(rsrc_t r, u32 voff, u32 soff) {
  return __builtin_bit_cast(u32x4, __builtin_amdgcn_raw_buffer_load_b128(r, voff, soff, 16));
}
__device__ __forceinline__ u32 ld4c(rsrc_t r, u32 voff, u32 soff) { return __builtin_amdgcn_raw_buffer_load_b32(r, voff, soff, 16); }
__device__ __forceinline__ void st16c(rsrc_t r, u32 voff, u32 soff, u32x4 v) { __builtin_amdgcn_raw_buffer_store_b128(v, r, voff, soff, 16); }
__device__ __forceinline__ void st8c(rsrc_t r, u32 voff, u32 soff, u32x2 v) { __builtin_amdgcn_raw_buffer_store_b64(v, r, voff, soff, 16); }
__device__ __forceinline__ void st4c(rsrc_t r, u32 voff, u32 soff, u32 v) { __builtin_amdgcn_raw_buffer_store_b32(v, r, voff, soff, 16); }

#define SPIN_MAX 200000
__device__ __forceinline__ void lds_flag_set(u32 addr, u32 val) {
  asm volatile("s_waitcnt lgkmcnt(0)\n\tds_write_b32 %0, %1" ::"v"(addr), "v"(val) : "memory");
}
#define LDS_LOOP for (;;)
__device__ __forceinline__ void lds_flag_wait1(u32 addr, u32 val) {
  LDS_LOOP {
    u32 f;
    asm volatile("ds_read_b32 %0, %1\n\ts_waitcnt lgkmcnt(0)" : "=v"(f) : "v"(addr) : "memory");
    if (__builtin_amdgcn_readfirstlane(f) >= val) break;
    __builtin_amdgcn_s_sleep(1);
  }
}
__device__ __forceinline__ void lds_flag_wait4(u32 base, u32 val) {
  LDS_LOOP {
    u32x4 f;
    asm volatile("ds_read_b128 %0, %1\n\ts_waitcnt lgkmcnt(0)" : "=v"(f) : "v"(base) : "memory");
    u32 m = min(min(f[0], f[1]), min(f[2], f[3]));
    if (__builtin_amdgcn_readfirstlane(m) >= val) break;
    __builtin_amdgcn_s_sleep(1);
  }
}
__device__ __forceinline__ void lds_bar4(u32 base, int w, u32 phase) {
  lds_flag_set(base + 4 * w, phase);
  lds_flag_wait4(base, phase);
}
__device__ __forceinline__ u32 lds_arrive(u32 addr) {
  u32 old = 0;
  if ((threadIdx.x & 63) == 0) {
    u32 one = 1u;
    asm volatile("s_waitcnt lgkmcnt(0)\n\tds_add_rtn_u32 %0, %1, %2\n\ts_waitcnt lgkmcnt(0)" : "=v"(old) : "v"(addr), "v"(one) : "memory");
  }
  return __builtin_amdgcn_readfirstlane(old);
}
__device__ __forceinline__ void poll4(const char* ws, u32 voff, u32 tag) {
  for (int it = 0; it < SPIN_MAX; it++) {
    u32 f;
    asm volatile("global_load_dword %0, %1, %2 sc1\n\ts_waitcnt vmcnt(0)" : "=v"(f) : "v"(voff), "s"(ws) : "memory");
    if (__all(f == tag)) break;
    __builtin_amdgcn_s_sleep(2);
  }
}
__device__ __forceinline__ void poll16(const char* ws, u32 voff, u32 tag) {
  for (int it = 0; it < SPIN_MAX; it++) {
    u32x4 f;
    asm volatile("global_load_dwordx4 %0, %1, %2 sc1\n\ts_waitcnt vmcnt(0)" : "=v"(f) : "v"(voff), "s"(ws) : "memory");
    const u32 a = f[0], b = f[1], c = f[2], d = f[3];
    if (__all(a == tag && b == tag && c == tag && d == tag)) break;
    __builtin_amdgcn_s_sleep(2);
  }
}

#define STAMP(kid, wv, i)
constexpr float QSCALE = 0.23570226039551584f * 1.4426950408889634f;

template <int CTRL>
__device__ __forceinline__ float dpp_add(float x) {
  return x + __builtin_bit_cast(float, __builtin_amdgcn_mov_dpp(__builtin_bit_cast(int, x), CTRL, 0xf, 0xf, true));
}
__device__ __forceinline__ float red8(float x) {
  x = dpp_add<0xB1>(x);
  x = dpp_add<0x4E>(x);
  x = dpp_add<0x141>(x);
  return x;
}

__device__ __forceinline__ float max16(const f32x16& s) {
  float a = fmaxf(fmaxf(s[0], s[1]), s[2]), b = fmaxf(fmaxf(s[3], s[4]), s[5]);
  float c = fmaxf(fmaxf(s[6], s[7]), s[8]), d = fmaxf(fmaxf(s[9], s[10]), s[11]);
  float e = fmaxf(fmaxf(s[12], s[13]), s[14]);
  return fmaxf(fmaxf(fmaxf(a, b), fmaxf(c, d)), fmaxf(e, s[15]));
}
template <int NB>
__device__ __forceinline__ void attn_body(char* __restrict__ ws, const rsrc_t rs, const u32 lds0, const int w, const int g,
                                          const int tg, const u32 tag, u32x4 (&Q1)[4], u32x4 (&Q2)[4]) {
  const int l = threadIdx.x & 63;
  const int h = l >> 5, c31 = l & 31;
  const int tt = w >> 1, hf = w & 1;
  const u32 flags = lds0 + FLAGS;
  const u32 k1v = K1F_OFF + l * 16;
  const u32 k2v = K2_OFF + c31 * 4;
  const u32 vv = VF_OFF + (h * 32 + (c31 < 18 ? c31 : 18)) * 16;
  const f32x16 zero16 = {};
  {
    u32x4 k1 = ld16c(rs, k1v, 0);
    u32x4 a2 = u32x4{ld4c(rs, k2v, 0), 0x3F803F80u, 0u, 0u};
#pragma unroll
    for (int i = 0; i < 4; i++) {
      f32x16 s = mfma32(k1, Q1[i], zero16);
      s = mfma32(a2, Q2[i], s);
      float m = max16(s);
      m = fmaxf(m, __shfl_xor(m, 32));
      float mr = m + 80.0f;
      float hi = rbf(mr), lo = rbf(mr - hi);
      Q2[i][1] = h ? 0u : cvtpk(-hi, -lo);
    }
  }
  STAMP(1, w + 4, 6);
  f32x16 O[4];
#pragma unroll
  for (int i = 0; i < 4; i++) O[i] = zero16;
  const int t0 = 32 * hf;
  u32x4 K1[NB], V0[NB], V1[NB];
  u32 K2[NB];
  auto loadK = [&](int bufi, int tile) {
    K1[bufi] = ld16c(rs, k1v, (u32)tile * 1024u);
    K2[bufi] = ld4c(rs, k2v, (u32)tile * 128u);
  };
  auto loadV = [&](int bufi, int tile) {
    V0[bufi] = ld16c(rs, vv, (u32)tile * 2048u);
    V1[bufi] = ld16c(rs, vv + 1024, (u32)tile * 2048u);
  };
#pragma unroll
  for (int j = 0; j < NB; j++) loadK(j, t0 + j);
#pragma unroll
  for (int j = 0; j < NB - 1; j++) loadV(j, t0 + j);
  f32x16 Sc = mfma32(K1[0], Q1[0], zero16);
  Sc = mfma32(u32x4{K2[0], 0x3F803F80u, 0u, 0u}, Q2[0], Sc);
  u32x4 Pp = {0u, 0u, 0u, 0u};
  V1[NB - 1] = u32x4{0u, 0u, 0u, 0u};
#pragma unroll 1
  for (int n0 = 0; n0 < 32; n0 += NB) {
#pragma unroll
    for (int u = 0; u < NB; u++) {
      const int n = n0 + u;
#pragma unroll
      for (int i = 0; i < 4; i++) {
        const int ni = (i + 1) & 3;
        const int nb = (i == 3) ? (u + 1) % NB : u;
        const int pi = (i + 3) & 3;
        const int pb = (i == 0) ? (u + NB - 1) % NB : u;
        f32x16 Sn = mfma32(K1[nb], Q1[ni], zero16);
        float e0 = __builtin_amdgcn_exp2f(Sc[0]), e1 = __builtin_amdgcn_exp2f(Sc[1]);
        float e2 = __builtin_amdgcn_exp2f(Sc[2]), e3 = __builtin_amdgcn_exp2f(Sc[3]);
        u32x4 P0, P1;
        P0[0] = cvtpk(e0, e1); P0[1] = cvtpk(e2, e3);
        SCHED_FENCE();
        Sn = mfma32(u32x4{K2[nb], 0x3F803F80u, 0u, 0u}, Q2[ni], Sn);
        e0 = __builtin_amdgcn_exp2f(Sc[4]); e1 = __builtin_amdgcn_exp2f(Sc[5]);
        e2 = __builtin_amdgcn_exp2f(Sc[6]); e3 = __builtin_amdgcn_exp2f(Sc[7]);
        P0[2] = cvtpk(e0, e1); P0[3] = cvtpk(e2, e3);
        SCHED_FENCE();
        O[pi] = mfma32(V1[pb], Pp, O[pi]);
        if (i == 0) loadV((u + NB - 1) % NB, (t0 + n + NB - 1) & 63);
        if (i == 3) loadK(u, (t0 + n + NB) & 63);
        e0 = __builtin_amdgcn_exp2f(Sc[8]); e1 = __builtin_amdgcn_exp2f(Sc[9]);
        e2 = __builtin_amdgcn_exp2f(Sc[10]); e3 = __builtin_amdgcn_exp2f(Sc[11]);
        P1[0] = cvtpk(e0, e1); P1[1] = cvtpk(e2, e3);
        SCHED_FENCE();
        O[i] = mfma32(V0[u], P0, O[i]);
        e0 = __builtin_amdgcn_exp2f(Sc[12]); e1 = __builtin_amdgcn_exp2f(Sc[13]);
        e2 = __builtin_amdgcn_exp2f(Sc[14]); e3 = __builtin_amdgcn_exp2f(Sc[15]);
        P1[2] = cvtpk(e0, e1); P1[3] = cvtpk(e2, e3);
        SCHED_FENCE();
        Pp = P1;
        Sc = Sn;
      }
    }
  }
  O[3] = mfma32(V1[NB - 1], Pp, O[3]);
  STAMP(1, w + 4, 7);
  f32x16 Ka, Kb, Sa, Sb;
  if (hf == 0) { Ka = O[0]; Kb = O[1]; Sa = O[2]; Sb = O[3]; }
  else { Ka = O[2]; Kb = O[3]; Sa = O[0]; Sb = O[1]; }
  lds_flag_wait4(flags + F_BAR, PH_DONE);
  const u32 xa = lds0 + XCH0 + l * 16;
#pragma unroll
  for (int j = 0; j < 3; j++) {
    lds_wr16(xa + ((w * 2 + 0) * 3 + j) * 1024, __builtin_bit_cast(u32x4, f32x4{Sa[4 * j], Sa[4 * j + 1], Sa[4 * j + 2], Sa[4 * j + 3]}));
    lds_wr16(xa + ((w * 2 + 1) * 3 + j) * 1024, __builtin_bit_cast(u32x4, f32x4{Sb[4 * j], Sb[4 * j + 1], Sb[4 * j + 2], Sb[4 * j + 3]}));
  }
  const int pw = w ^ 1;
  lds_flag_set(flags + F_PX + 4 * w, 1u);
  lds_flag_wait1(flags + F_PX + 4 * pw, 1u);
#pragma unroll
  for (int j = 0; j < 3; j++) {
    f32x4 ra = __builtin_bit_cast(f32x4, lds_rd16(xa + ((pw * 2 + 0) * 3 + j) * 1024));
    f32x4 rb = __builtin_bit_cast(f32x4, lds_rd16(xa + ((pw * 2 + 1) * 3 + j) * 1024));
#pragma unroll
    for (int r = 0; r < 4; r++) { Ka[4 * j + r] += ra[r]; Kb[4 * j + r] += rb[r]; }
  }
  STAMP(1, w + 4, 8);
  {
    float z = 0.f;
#pragma unroll
    for (int r = 0; r < 12; r++) { z = fmaf(Ka[r], 0.f, z); z = fmaf(Kb[r], 0.f, z); }
    bool bad = !(z == 0.f) || !(Ka[10] >= 8e-28f) || !(Kb[10] >= 8e-28f);
    if (__any(bad)) {
      u32x4 q1a, q2a, q1b, q2b;
      if (hf == 0) { q1a = Q1[0]; q2a = Q2[0]; q1b = Q1[1]; q2b = Q2[1]; }
      else { q1a = Q1[2]; q2a = Q2[2]; q1b = Q1[3]; q2b = Q2[3]; }
      q2a[1] = 0u; q2b[1] = 0u;
      float ma = -INFINITY, mb = -INFINITY;
#pragma unroll 1
      for (int t = 0; t < 64; t++) {
        u32x4 k1 = ld16c(rs, k1v, (u32)t * 1024u);
        u32x4 a2 = u32x4{ld4c(rs, k2v, (u32)t * 128u), 0x3F803F80u, 0u, 0u};
        f32x16 s = mfma32(k1, q1a, zero16); s = mfma32(a2, q2a, s);
        ma = fmaxf(ma, max16(s));
        s = mfma32(k1, q1b, zero16); s = mfma32(a2, q2b, s);
        mb = fmaxf(mb, max16(s));
      }
      ma = fmaxf(ma, __shfl_xor(ma, 32));
      mb = fmaxf(mb, __shfl_xor(mb, 32));
      Ka = zero16; Kb = zero16;
#pragma unroll 1
      for (int t = 0; t < 64; t++) {
        u32x4 k1 = ld16c(rs, k1v, (u32)t * 1024u);
        u32x4 a2 = u32x4{ld4c(rs, k2v, (u32)t * 128u), 0x3F803F80u, 0u, 0u};
        u32x4 v0 = ld16c(rs, vv, (u32)t * 2048u), v1 = ld16c(rs, vv + 1024, (u32)t * 2048u);
        f32x16 s = mfma32(k1, q1a, zero16); s = mfma32(a2, q2a, s);
        u32x4 p0, p1;
#pragma unroll
        for (int r = 0; r < 4; r++) {
          p0[r] = cvtpk(__builtin_amdgcn_exp2f(s[2 * r] - ma), __builtin_amdgcn_exp2f(s[2 * r + 1] - ma));
          p1[r] = cvtpk(__builtin_amdgcn_exp2f(s[8 + 2 * r] - ma), __builtin_amdgcn_exp2f(s[9 + 2 * r] - ma));
        }
        Ka = mfma32(v0, p0, Ka); Ka = mfma32(v1, p1, Ka);
        s = mfma32(k1, q1b, zero16); s = mfma32(a2, q2b, s);
#pragma unroll
        for (int r = 0; r < 4; r++) {
          p0[r] = cvtpk(__builtin_amdgcn_exp2f(s[2 * r] - mb), __builtin_amdgcn_exp2f(s[2 * r + 1] - mb));
          p1[r] = cvtpk(__builtin_amdgcn_exp2f(s[8 + 2 * r] - mb), __builtin_amdgcn_exp2f(s[9 + 2 * r] - mb));
        }
        Kb = mfma32(v0, p0, Kb); Kb = mfma32(v1, p1, Kb);
      }
    }
  }
  {
    const float ia = 1.0f / Ka[10], ib = 1.0f / Kb[10];
    u32x4 oa, ob;
#pragma unroll
    for (int r = 0; r < 4; r++) {
      oa[r] = cvtpk(Ka[2 * r] * ia, Ka[2 * r + 1] * ia);
      ob[r] = cvtpk(Kb[2 * r] * ib, Kb[2 * r + 1] * ib);
    }
    const u32 so = OF_OFF + (u32)((tg * 36 + 4 * g + 2 * hf) * 2 + tt) * 1024u;
    st16c(rs, l * 16, so, oa);
    st16c(rs, l * 16, so + 2048, ob);
    if (h == 0) {
      u32x2 ex = {cvtpk(Ka[8] * ia, Ka[9] * ia), cvtpk(Kb[8] * ib, Kb[9] * ib)};
      st8c(rs, ((g & 1) * 32 + c31) * 16 + 8 * hf, OF_OFF + (u32)((tg * 36 + 32 + (g >> 1)) * 2 + tt) * 1024u, ex);
    }
    waitvm<0>();
    if (lds_arrive(flags + F_OCNT) == 3) {
      if (l == 0) st4c(rs, OFL_OFF + g * 4, (u32)tg * 128u, tag);
    }
  }
  STAMP(1, w + 4, 9);
}

struct FfnLd {
  const char* s0;
  const char* s1;
  const char* sx;
  u32 vA, vB, vX;
};

__device__ __forceinline__ void glds_first(u32 m0val, u32 voff, const char* sbase) {
  asm volatile("s_mov_b32 m0, %0\n\ts_nop 0\n\tglobal_load_lds_dwordx4 %1, %2 offset:-2048" ::"s"(m0val), "v"(voff), "s"(sbase) : "memory");
}
template <int OFF>
__device__ __forceinline__ void glds_next(u32 voff, const char* sbase) {
  if constexpr (OFF == -1024) asm volatile("global_load_lds_dwordx4 %0, %1 offset:-1024" ::"v"(voff), "s"(sbase) : "memory");
  else if constexpr (OFF == 0) asm volatile("global_load_lds_dwordx4 %0, %1" ::"v"(voff), "s"(sbase) : "memory");
  else if constexpr (OFF == 1024) asm volatile("global_load_lds_dwordx4 %0, %1 offset:1024" ::"v"(voff), "s"(sbase) : "memory");
  else asm volatile("global_load_lds_dwordx4 %0, %1 offset:2048" ::"v"(voff), "s"(sbase) : "memory");
}
__device__ __forceinline__ void ffn_issue_stage(u32 m0val, const FfnLd& ld) {
  glds_first(m0val, ld.vA, ld.s0);
  glds_next<-1024>(ld.vB, ld.s0);
  glds_next<0>(ld.vA, ld.s1);
  glds_next<1024>(ld.vB, ld.s1);
  glds_next<2048>(ld.vX, ld.sx);
}

#define YMASK 0
#define FHI 2
template <int I>
__device__ __forceinline__ void fprio() {
  if constexpr (YMASK != 0) {
    constexpr bool lo = (YMASK >> I) & 1;
    constexpr bool plo = (YMASK >> ((I + 8) % 9)) & 1;
    if constexpr (lo != plo) { __builtin_amdgcn_s_setprio(lo ? 0 : FHI); __builtin_amdgcn_sched_barrier(0); }
  }
}
template <int SL, int WAITN>
__device__ __forceinline__ void ffn_step(f32x16 (&acc)[8], f32x16& accx, u32x4 (&A)[5], u32x4 (&Bc)[2], u32 aAddr0,
                                         u32 aAddr1, u32 xAddr0, u32 xAddr1, u32 bAddr0, u32 bAddr1, u32 ringm0,
                                         FfnLd& ld, u32 kstep) {
  constexpr u32 NS = ((SL + 1) & 3) * SLOT;
  waitvm<WAITN>();
  SCHED_FENCE();
  fprio<0>();
  acc[0] = mfma32(A[0], Bc[0], acc[0]);
  u32x4 Bn0 = lds_rd16(bAddr0), Bn1 = lds_rd16(bAddr1);
  SCHED_FENCE();
  fprio<1>();
  acc[1] = mfma32(A[0], Bc[1], acc[1]);
  A[0] = tr2(aAddr0 + NS, aAddr1 + NS);
  SCHED_FENCE();
  fprio<2>();
  acc[2] = mfma32(A[1], Bc[0], acc[2]);
  SCHED_FENCE();
  fprio<3>();
  acc[3] = mfma32(A[1], Bc[1], acc[3]);
  A[1] = tr2((aAddr0 ^ 64) + NS, (aAddr1 ^ 64) + NS);
  SCHED_FENCE();
  fprio<4>();
  acc[4] = mfma32(A[2], Bc[0], acc[4]);
  glds_first(ringm0 + SL * SLOT, ld.vA, ld.s0);
  SCHED_FENCE();
  fprio<5>();
  acc[5] = mfma32(A[2], Bc[1], acc[5]);
  A[2] = tr2(aAddr0 + NS + 2048, aAddr1 + NS + 2048);
  glds_next<-1024>(ld.vB, ld.s0);
  SCHED_FENCE();
  fprio<6>();
  acc[6] = mfma32(A[3], Bc[0], acc[6]);
  SCHED_FENCE();
  fprio<7>();
  acc[7] = mfma32(A[3], Bc[1], acc[7]);
  A[3] = tr2((aAddr0 ^ 64) + NS + 2048, (aAddr1 ^ 64) + NS + 2048);
  glds_next<0>(ld.vA, ld.s1);
  SCHED_FENCE();
  fprio<8>();
  accx = mfma32(A[4], Bc[0], accx);
  A[4] = tr2(xAddr0 + NS, xAddr1 + NS);
  glds_next<1024>(ld.vB, ld.s1);
  glds_next<2048>(ld.vX, ld.sx);
  ld.vA += kstep; ld.vB += kstep; ld.vX += kstep;
  SCHED_FENCE();
  Bc[0] = Bn0; Bc[1] = Bn1;
}

__device__ __forceinline__ void ffn_body(const u16* __restrict__ wo, const u16* __restrict__ w1, const u16* __restrict__ w3,
                                         const u16* __restrict__ w2, char* __restrict__ ws, const rsrc_t rs, const u32 lds0,
                                         const int w, const int g, const int tg, const int rot, const u32 tag) {
  int l = threadIdx.x & 63;
  const int tl0 = w >> 1;
  const u32 flags = lds0 + FLAGS;
  const u32 ring = lds0 + RING0 + w * STG;
  const u32 ringm0 = ring + 2048;

  u32x4 q1[2];
  u32 q2[2];
  {
    const int h = l >> 5, G1 = (l >> 4) & 1, q4 = (l & 15) >> 2, p4 = l & 3;
    const int s = 4 * G1 + p4;
    const u32 offE = s == 0 ? 0 : s == 1 ? 16 : s == 2 ? 8 : s == 3 ? 24 : 32;
    const u32 offO = s == 0 ? 0 : s == 1 ? 16 : s == 2 ? 8 : s == 3 ? 24 : s == 4 ? 16 : 32;
    const u32 qa = lds0 + RING0 + (8 * h + q4) * 144 + ((w & 1) ? 36 * w - 4 + offO : 36 * w + offE);
    const u32 xb = lds0 + h * 512 + (((l & 31) ^ h) * 16);
    f32x16 qacc[2] = {f32x16{}, f32x16{}};
#pragma unroll
    for (int ks = 0; ks < 36; ks++) {
      u32x4 aop = tr2(qa + ks * 2304, qa + ks * 2304 + 576);
      const u32 xa = (xb ^ (32 * (ks & 3))) + ks * 2048;
      qacc[0] = mfma32(aop, lds_rd16(xa), qacc[0]);
      qacc[1] = mfma32(aop, lds_rd16(xa + 1024), qacc[1]);
    }
#pragma unroll
    for (int t = 0; t < 2; t++) {
      float v[10];
      if (w & 1) {
#pragma unroll
        for (int r = 0; r < 8; r++) v[r] = qacc[t][2 + r];
        v[8] = qacc[t][14]; v[9] = qacc[t][15];
      } else {
#pragma unroll
        for (int r = 0; r < 10; r++) v[r] = qacc[t][r];
      }
#pragma unroll
      for (int r = 0; r < 4; r++) q1[t][r] = cvtpk(rbf(v[2 * r]) * QSCALE, rbf(v[2 * r + 1]) * QSCALE);
      q2[t] = cvtpk(rbf(v[8]) * QSCALE, rbf(v[9]) * QSCALE);
    }
  }
  STAMP(1, w, 3);
  lds_bar4(flags + F_BAR, w, 1u);
  FfnLd ld;
  auto set_lane_offsets = [&](u32 strd, int kst) {
    int lo = l;
    asm volatile("" : "+v"(lo));
    const int r = lo >> 3, c = (lo & 7) ^ (((r >> 1) & 1) << 2);
    const u32 koff = (u32)kst * 16u * strd;
    ld.vA = r * strd + c * 16 + koff;
    ld.vB = ld.vA + 8 * strd - 1024;
    ld.vX = (lo >> 2) * strd + (lo & 3) * 16 + koff;
  };
  auto set_bases = [&](int pass) {
    if (pass < 2) {
      const size_t co = (size_t)g * 1152 + (size_t)(2 * w + pass) * 128;
      ld.s0 = (const char*)w1 + co + 2048;
      ld.s1 = (const char*)w3 + co;
      ld.sx = (const char*)(pass == 0 ? w1 : w3) + (size_t)g * 1152 + 1024 + (w & 1) * 64 - 2048;
    } else {
      const char* base = (const char*)w2 + (size_t)g * 576 * 1152;
      ld.s0 = base + w * 256 + 2048;
      ld.s1 = base + w * 256 + 128;
      ld.sx = base + 1024 + (w & 1) * 64 - 2048;
    }
  };
  set_bases(0);
  set_lane_offsets(9216, rot);
#pragma unroll
  for (int s = 0; s < 3; s++) {
    ffn_issue_stage(ringm0 + s * SLOT, ld);
    ld.vA += 16 * 9216; ld.vB += 16 * 9216; ld.vX += 16 * 9216;
  }
  {
    const u32 qb = lds0 + QSLOT + w * STG;
    lds_wr16(qb + l * 16, q1[0]);
    lds_wr16(qb + 1152 + l * 16, q1[1]);
    if (l < 32) { lds_wr4(qb + 1024 + l * 4, q2[0]); lds_wr4(qb + 1152 + 1024 + l * 4, q2[1]); }
    lds_flag_set(flags + F_QRD + 4 * w, 1u);
    lds_flag_wait4(flags + F_QCN, 1u);
  }
  ffn_issue_stage(ringm0 + 3 * SLOT, ld);
  ld.vA += 16 * 9216; ld.vB += 16 * 9216; ld.vX += 16 * 9216;
  STAMP(1, w, 4);

  f32x16 acc[8], accx;
  u32x4 GX[2];
  u32x4 A[5], Bc[2];
  u32x4 H0[4][2];
  const f32x16 zero16 = {};
#pragma unroll
  for (int t = 0; t < 4; t++) { H0[t][0] = u32x4{0, 0, 0, 0}; H0[t][1] = u32x4{0, 0, 0, 0}; }
  GX[0] = u32x4{0, 0, 0, 0}; GX[1] = u32x4{0, 0, 0, 0};

#pragma unroll 1
  for (int pass = 0; pass < 3; pass++) {
    u32 a0, a1, x0, x1, bL0, bL1;
    {
      int lo = l;
      asm volatile("" : "+v"(lo));
      const int h = lo >> 5, G1 = (lo >> 4) & 1, q4 = (lo & 15) >> 2, p4 = lo & 3;
      const u32 swz = ((q4 >> 1) & 1) * 64;
      const int hm = pass < 2 ? 8 : 4, tm = 12 - hm;
      a0 = ring + (hm * h + q4) * 128 + ((G1 * 32 + 8 * p4) ^ swz); a1 = a0 + tm * 128;
      x0 = ring + 4096 + (hm * h + q4) * 64 + G1 * 32 + 8 * p4; x1 = x0 + tm * 64;
      const u32 bl = lds0 + h * 512 + (((lo & 31) ^ h) * 16);
      bL0 = bl + tl0 * 1024; bL1 = bl + (1 - tl0) * 1024;
    }
#pragma unroll
    for (int i = 0; i < 8; i++) acc[i] = zero16;
    accx = zero16;
    Bc[0] = lds_rd16(bL0 + rot * 2048);
    Bc[1] = lds_rd16(bL1 + rot * 2048);
    waitvm<15>();
    SCHED_FENCE();
    A[0] = tr2(a0, a1);
    SCHED_FENCE();
    A[1] = tr2(a0 ^ 64, a1 ^ 64);
    SCHED_FENCE();
    A[2] = tr2(a0 + 2048, a1 + 2048);
    SCHED_FENCE();
    A[3] = tr2((a0 ^ 64) + 2048, (a1 ^ 64) + 2048);
    SCHED_FENCE();
    A[4] = tr2(x0, x1);
    SCHED_FENCE();
    const u32 strd = pass < 2 ? 9216u : 1152u;
    u32 kstep = 16u * strd;
    int kb = rot;
    STAMP(1, w, 10 + pass);
#pragma unroll 1
    for (int j = 0; j < 9; j++) {
      int kn = kb + 4;
      if (kn == 36) kn = 0;
      if (j == 8) {
        if (pass < 2) {
          const u32 st2 = pass == 0 ? 9216u : 1152u;
          set_bases(pass + 1); set_lane_offsets(st2, rot); kstep = 16u * st2;
        } else {
          ld.vA = 0; ld.vB = 0; ld.vX = 2048; kstep = 0;
        }
      } else if (kn == 0) {
        set_lane_offsets(strd, 0);
      }
      const u32 bb0 = bL0 + kb * 2048, bb1 = bL1 + kb * 2048;
      ffn_step<0, 10>(acc, accx, A, Bc, a0, a1, x0, x1, (bb0 ^ 32) + 2048, (bb1 ^ 32) + 2048, ringm0, ld, kstep);
      ffn_step<1, 10>(acc, accx, A, Bc, a0, a1, x0, x1, (bb0 ^ 64) + 4096, (bb1 ^ 64) + 4096, ringm0, ld, kstep);
      ffn_step<2, 10>(acc, accx, A, Bc, a0, a1, x0, x1, (bb0 ^ 96) + 6144, (bb1 ^ 96) + 6144, ringm0, ld, kstep);
      ffn_step<3, 10>(acc, accx, A, Bc, a0, a1, x0, x1, bL0 + kn * 2048, bL1 + kn * 2048, ringm0, ld, kstep);
      kb = kn;
    }
    if (pass == 0) {
#pragma unroll
      for (int t = 0; t < 4; t++)
#pragma unroll
        for (int s = 0; s < 2; s++) {
#pragma unroll
          for (int r = 0; r < 4; r++) {
            const int e = 8 * s + 2 * r;
            H0[t][s][r] = cvtpk(silu_f(acc[t][e]) * acc[4 + t][e], silu_f(acc[t][e + 1]) * acc[4 + t][e + 1]);
          }
          SCHED_FENCE();
        }
#pragma unroll
      for (int e = 0; e < 8; e++) GX[e >> 2][e & 3] = cvtpk(silu_f(accx[2 * e]), silu_f(accx[2 * e + 1]));
    } else if (pass == 1) {
      lds_bar4(flags + F_BAR, w, 2u);
#pragma unroll
      for (int t = 0; t < 4; t++)
#pragma unroll
        for (int s = 0; s < 2; s++) {
          const int ft = t >> 1, ltt = t & 1;
          lds_wr16(((ltt == 0 ? bL0 : bL1) ^ (32 * ((2 * ft + s) & 3))) + (8 * w + 2 * ft + s) * 2048, H0[t][s]);
        }
      SCHED_FENCE();
#pragma unroll
      for (int t = 0; t < 4; t++)
#pragma unroll
        for (int s = 0; s < 2; s++) {
          u32x4 hh;
#pragma unroll
          for (int r = 0; r < 4; r++) {
            const int e = 8 * s + 2 * r;
            hh[r] = cvtpk(silu_f(acc[t][e]) * acc[4 + t][e], silu_f(acc[t][e + 1]) * acc[4 + t][e + 1]);
          }
          const int ft = t >> 1, ltt = t & 1;
          lds_wr16(((ltt == 0 ? bL0 : bL1) ^ (32 * ((2 * ft + s) & 3))) + (8 * w + 4 + 2 * ft + s) * 2048, hh);
          SCHED_FENCE();
        }
#pragma unroll
      for (int s = 0; s < 2; s++) {
        u32x4 hx;
#pragma unroll
        for (int r = 0; r < 4; r++) {
          const int e = 8 * s + 2 * r;
          hx[r] = cvtpk(lo2f(GX[s][r]) * accx[e], hi2f(GX[s][r]) * accx[e + 1]);
        }
        lds_wr16((bL0 ^ (32 * ((2 * (w & 1) + s) & 3))) + (32 + 2 * (w & 1) + s) * 2048, hx);
      }
      lds_bar4(flags + F_BAR, w, 3u);
    }
  }
  STAMP(1, w, 5);
  __builtin_amdgcn_s_setprio(0);
  waitvm<0>();
  lds_bar4(flags + F_BAR, w, PH_DONE);
  {
    int lo = l;
    asm volatile("" : "+v"(lo));
    const u32 so = PART_OFF + (u32)(g * 32 + tg) * 73728u;
#pragma unroll
    for (int t = 0; t < 8; t++) {
      const int dt = 4 * w + (t >> 1);
      const int tt = (t & 1) == 0 ? tl0 : 1 - tl0;
#pragma unroll
      for (int hh = 0; hh < 2; hh++) {
        u32x4 o;
#pragma unroll
        for (int r = 0; r < 4; r++) o[r] = cvtpk(acc[t][8 * hh + 2 * r], acc[t][8 * hh + 2 * r + 1]);
        st16c(rs, lo * 16, so + (u32)((dt * 2 + tt) * 2 + hh) * 1024u, o);
      }
    }
#pragma unroll
    for (int hh = 0; hh < 2; hh++) {
      u32x4 o;
#pragma unroll
      for (int r = 0; r < 4; r++) o[r] = cvtpk(accx[8 * hh + 2 * r], accx[8 * hh + 2 * r + 1]);
      st16c(rs, lo * 16, so + (u32)(((16 + (w & 1)) * 2 + tl0) * 2 + hh) * 1024u, o);
    }
    waitvm<0>();
    if (lds_arrive(flags + F_PCNT) == 3) {
      if (lo == 0) st4c(rs, PFL_OFF + g * 4, (u32)tg * 128u, tag);
    }
    STAMP(1, w, 6);
    {
      const int c = (lo & 7) ^ (((lo >> 4) & 1) << 2);
      u32 vo = (lo >> 3) * 1152 + (c >> 2 ? 64 * (g + 8) : 64 * g) + (c & 3) * 16 + w * 18 * 9216;
#pragma unroll 1
      for (int i = 0; i < 18; i++) { glds(lds0 + (w * 18 + i) * 1024, vo, wo); vo += 9216; }
      u32 ve = (lo >> 1) * 1152 + 1024 + 32 * (g & 3) + (lo & 1) * 16;
#pragma unroll 1
      for (int i = w; i < 18; i += 4) glds(lds0 + WOX0 + i * 1024, ve + i * 32 * 1152, wo);
    }
    if (w == 0) {
      poll4(ws, PFL_OFF + tg * 128 + (lo & 7) * 4, tag);
      lds_flag_set(flags + F_PRD, 1u);
    } else {
      lds_flag_wait1(flags + F_PRD, 1u);
    }
    STAMP(1, w, 7);
    {
      const int tt = w >> 1, dt = g + 8 * (w & 1);
      const u32 sp = PART_OFF + (u32)tg * 73728u + (u32)((dt * 2 + tt) * 2) * 1024u;
      u32x4 P[2][8];
#pragma unroll
      for (int hh = 0; hh < 2; hh++)
#pragma unroll
        for (int gg = 0; gg < 8; gg++) P[hh][gg] = ld16c(rs, lo * 16, sp + (u32)hh * 1024u + (u32)gg * 32u * 73728u);
      u32x4 PX[8];
      if (w == 3) {
        const u32 sx = PART_OFF + (u32)tg * 73728u + (u32)(((16 + ((g & 3) >> 1)) * 2 + (g >> 2)) * 2 + (g & 1)) * 1024u;
#pragma unroll
        for (int gg = 0; gg < 8; gg++) PX[gg] = ld16c(rs, lo * 16, sx + (u32)gg * 32u * 73728u);
      }
      auto gate = [&](const u32x4(&p)[8]) {
        u32x4 o;
#pragma unroll
        for (int r = 0; r < 4; r++) {
          float f0 = 0.f, f1 = 0.f;
#pragma unroll
          for (int gg = 0; gg < 8; gg++) { f0 += lo2f(p[gg][r]); f1 += hi2f(p[gg][r]); }
          o[r] = cvtpk(silu_f(rbf(f0)), silu_f(rbf(f1)));
        }
        return o;
      };
#pragma unroll
      for (int hh = 0; hh < 2; hh++) lds_wr16(lds0 + GG0 + (tt * 4 + (w & 1) * 2 + hh) * 1024 + lo * 16, gate(P[hh]));
      if (w == 3) lds_wr16(lds0 + GG0 + 8 * 1024 + lo * 16, gate(PX));
    }
    STAMP(1, w, 8);
    waitvm<0>();
    if (w == 0) poll4(ws, OFL_OFF + tg * 128 + (lo & 7) * 4, tag);
    STAMP(1, w, 9);
  }
}

#define PRIO_FFN 3
#define PRIO_ATT 1
#define XMAP 2
__global__ __launch_bounds__(512, 2) void fused_kernel(const u16* __restrict__ x, const u16* __restrict__ wq,
                                                       const u16* __restrict__ wk, const u16* __restrict__ wv,
                                                       const u16* __restrict__ wo, const u16* __restrict__ w1,
                                                       const u16* __restrict__ w3, const u16* __restrict__ w2,
                                                       char* __restrict__ ws, u16* __restrict__ out) {
  extern __shared__ __attribute__((aligned(1024))) char smem[];
  const u32 lds0 = (u32)(uintptr_t)(LDS_AS char*)smem;
  const u32 flags = lds0 + FLAGS;
  const int b = blockIdx.x, xcd = b & 7, jj = b >> 3;
  const int g = (xcd & 1) * 4 + (jj & 3), tg = (xcd >> 1) * 8 + (jj >> 2);
#define ROTEXPR ((g * 8) % 36)
  const int rot = ROTEXPR;
  const int w = __builtin_amdgcn_readfirstlane(threadIdx.x >> 6);
  const int l = threadIdx.x & 63;
  const rsrc_t rs = __builtin_amdgcn_make_buffer_rsrc((void*)ws, (short)0, (int)WS_END, 0x00020000);
  STAMP(1, w, 0);

  u32 ep;
  {
    u32 zero = 0;
    asm volatile("global_load_dword %0, %1, %2 sc1" : "=v"(ep) : "v"(zero), "s"(ws) : "memory");
  }
  if (w == 0) {
    const int n = 80 * 64 + l, row = (n * 7282) >> 16;
    glds(lds0 + RING0 + 80 * 1024, (u32)(row * 1152 + (n - row * 9) * 16), (const char*)wq + 144 * g);
  }
  u32x4 xv[9];
  {
    const u32 xo = (u32)((64 * tg + 8 * w + (l >> 3)) * 1152 + (l & 7) * 16);
#define XLD(i) asm volatile("global_load_dwordx4 %0, %1, %2 offset:" #i "*128" : "=v"(xv[i]) : "v"(xo), "s"(x) : "memory")
    XLD(0); XLD(1); XLD(2); XLD(3); XLD(4); XLD(5); XLD(6); XLD(7); XLD(8);
#undef XLD
  }
  u32 wg[36];
  if (w < 4) {
#pragma unroll
    for (int j = 0; j < 20; j++) {
      const int i = w + 4 * j, n = i * 64 + l, row = (n * 7282) >> 16;
      glds(lds0 + RING0 + i * 1024, (u32)(row * 1152 + (n - row * 9) * 16), (const char*)wq + 144 * g);
    }
    if (threadIdx.x < 32) lds_wr4(flags + 4 * threadIdx.x, 0u);
    waitvm<20>();
  } else {
    const u16* wsrc = g < 4 ? wk : wv;
    const int i = l & 15, kg = l >> 4, pi = i < 8 ? i : 8;
    const u32 o0 = (u32)((144 * (w - 4) + 4 * kg) * 36 + 4 * pi), o1 = o0 + 64 * 36;
#define GLD(s, t) asm volatile("global_load_dword %0, %1, %2 offset:%3" : "=v"(wg[(s) * 4 + (t)]) : "v"((s) < 4 ? o0 : o1), "s"(wsrc), "n"((((s) < 4 ? (s) : (s) - 4) * 16 + (t)) * 36) : "memory")
#define GLD4(s) GLD(s, 0); GLD(s, 1); GLD(s, 2); GLD(s, 3)
    GLD4(0); GLD4(1); GLD4(2); GLD4(3); GLD4(4); GLD4(5); GLD4(6); GLD4(7); GLD4(8);
#undef GLD4
#undef GLD
    waitvm<36>();
  }
  asm volatile("" : "+v"(xv[0]), "+v"(xv[1]), "+v"(xv[2]), "+v"(xv[3]), "+v"(xv[4]), "+v"(xv[5]), "+v"(xv[6]), "+v"(xv[7]), "+v"(xv[8]), "+v"(ep));
  const u32 tag = __builtin_amdgcn_readfirstlane(ep) + 1u;
  STAMP(1, w, 1);
  {
    u32 ones_u = 0x3F803F80u;
    asm volatile("" : "+v"(ones_u));
    float s0 = 0, s1 = 0, s2 = 0, s3 = 0;
#pragma unroll
    for (int i = 0; i < 9; i++) {
      s0 = dot2bf(xv[i][0], ones_u, s0);
      s1 = dot2bf(xv[i][1], ones_u, s1);
      s2 = dot2bf(xv[i][2], ones_u, s2);
      s3 = dot2bf(xv[i][3], ones_u, s3);
    }
    const float sum = red8((s0 + s1) + (s2 + s3));
    const float mean = sum * (1.0f / 576.0f);
    const float mb = rbf(mean);
    s0 = s1 = s2 = s3 = 0;
#pragma unroll
    for (int i = 0; i < 9; i++) {
#pragma unroll
      for (int k = 0; k < 4; k += 2) {
        float a0 = lo2f(xv[i][k]) - mb, a1 = hi2f(xv[i][k]) - mb;
        float a2 = lo2f(xv[i][k + 1]) - mb, a3 = hi2f(xv[i][k + 1]) - mb;
        s0 = fmaf(a0, a0, s0); s1 = fmaf(a1, a1, s1); s2 = fmaf(a2, a2, s2); s3 = fmaf(a3, a3, s3);
        xv[i][k] = cvtpk(a0, a1);
        xv[i][k + 1] = cvtpk(a2, a3);
      }
    }
    const float sq = red8((s0 + s1) + (s2 + s3));
    const float dm = mean - mb;
    const float var = rbf(fmaxf(sq * (1.0f / 576.0f) - dm * dm, 0.0f));
    const float den = rbf(sqrtf(rbf(var + 1e-5f)));
    const float rden = 1.0f / den;
    const int kg = l & 7, tok32 = 8 * (w & 3) + (l >> 3);
    const u32 xw = lds0 + (kg >> 1) * 2048 + (w >> 2) * 1024 + (kg & 1) * 512 + ((tok32 ^ kg) * 16);
#pragma unroll
    for (int i = 0; i < 9; i++) {
      u32x4 o;
#pragma unroll
      for (int k = 0; k < 4; k++) o[k] = cvtpk(lo2f(xv[i][k]) * rden, hi2f(xv[i][k]) * rden);
      lds_wr16(xw + i * 8192, o);
    }
  }
  STAMP(1, w, 2);
  waitvm<0>();
  rawbar();

  if (w < 4) {
    __builtin_amdgcn_s_setprio(PRIO_FFN);
    ffn_body(wo, w1, w3, w2, ws, rs, lds0, w, g, tg, rot, tag);
  } else {
    const int aw = w - 4;
    {
      asm volatile("" : "+v"(wg[0]), "+v"(wg[1]), "+v"(wg[2]), "+v"(wg[3]), "+v"(wg[4]), "+v"(wg[5]), "+v"(wg[6]), "+v"(wg[7]), "+v"(wg[8]),
                   "+v"(wg[9]), "+v"(wg[10]), "+v"(wg[11]), "+v"(wg[12]), "+v"(wg[13]), "+v"(wg[14]), "+v"(wg[15]), "+v"(wg[16]), "+v"(wg[17]));
      asm volatile("" : "+v"(wg[18]), "+v"(wg[19]), "+v"(wg[20]), "+v"(wg[21]), "+v"(wg[22]), "+v"(wg[23]), "+v"(wg[24]), "+v"(wg[25]), "+v"(wg[26]),
                   "+v"(wg[27]), "+v"(wg[28]), "+v"(wg[29]), "+v"(wg[30]), "+v"(wg[31]), "+v"(wg[32]), "+v"(wg[33]), "+v"(wg[34]), "+v"(wg[35]));
      const int T16 = g & 3, i = l & 15, kg = l >> 4, hh = kg >> 1;
      const int tok32 = 16 * (T16 & 1) + i;
      const u32 xb = lds0 + (T16 >> 1) * 1024 + hh * 512 + ((tok32 ^ hh) * 16) + (kg & 1) * 8;
      f32x4 E = {0, 0, 0, 0}, O = {0, 0, 0, 0};
      const bool isK = g < 4;
#pragma unroll
      for (int s = 0; s < 9; s++) {
        const int ks = 9 * aw + s;
        const u32x2 xo = lds_rd8((xb ^ (32 * (ks & 3))) + ks * 2048);
        const u32x2 we = {__builtin_amdgcn_perm(wg[4 * s + 1], wg[4 * s], 0x05040100u), __builtin_amdgcn_perm(wg[4 * s + 3], wg[4 * s + 2], 0x05040100u)};
        const u32x2 wd = {__builtin_amdgcn_perm(wg[4 * s + 1], wg[4 * s], 0x07060302u), __builtin_amdgcn_perm(wg[4 * s + 3], wg[4 * s + 2], 0x07060302u)};
        if (isK) { E = mfma16k16(we, xo, E); O = mfma16k16(wd, xo, O); }
        else { E = mfma16k16(xo, we, E); O = mfma16k16(xo, wd, O); }
      }
      if (aw != 0) {
        lds_wr16(lds0 + KVRED + ((aw - 1) * 2 + 0) * 1024 + l * 16, __builtin_bit_cast(u32x4, E));
        lds_wr16(lds0 + KVRED + ((aw - 1) * 2 + 1) * 1024 + l * 16, __builtin_bit_cast(u32x4, O));
        lds_flag_set(flags + F_KVP + 4 * aw, 1u);
      } else {
        lds_flag_set(flags + F_KVP, 1u);
        lds_flag_wait4(flags + F_KVP, 1u);
#pragma unroll
        for (int j = 0; j < 3; j++) {
          E += __builtin_bit_cast(f32x4, lds_rd16(lds0 + KVRED + (j * 2 + 0) * 1024 + l * 16));
          O += __builtin_bit_cast(f32x4, lds_rd16(lds0 + KVRED + (j * 2 + 1) * 1024 + l * 16));
        }
        const int t16 = 4 * tg + T16, rg = kg;
        if (isK) {
          const u32x4 kv = {cvtpk(E[0], O[0]), cvtpk(E[1], O[1]), cvtpk(E[2], O[2]), cvtpk(E[3], O[3])};
          if (rg < 2) st16c(rs, K1F_OFF + (rg * 32 + 16 * (t16 & 1) + i) * 16, (u32)(t16 >> 1) * 1024u, kv);
          else if (rg == 2) st4c(rs, K2_OFF + i * 4, (u32)t16 * 64u, kv[0]);
        } else {
          const u32 vb = VF_OFF + (rg & 1) * 512 + (rg >> 1) * 8;
          u32x2 e = {cvtpk(E[0], E[1]), cvtpk(E[2], E[3])}, o = {cvtpk(O[0], O[1]), cvtpk(O[2], O[3])};
          if (i == 9) e = u32x2{0x3F803F80u, 0x3F803F80u};
          if (i <= 9) st8c(rs, vb + 2 * i * 16, (u32)t16 * 1024u, e);
          if (i <= 8) st8c(rs, vb + (2 * i + 1) * 16, (u32)t16 * 1024u, o);
        }
        waitvm<0>();
        if (l < KVREP) st4c(rs, KVF_OFF + l * 1024 + b * 4, 0, tag);
      }
    }
    STAMP(1, w, 3);
    u32x4 Q1[4], Q2[4];
    {
      lds_flag_wait4(flags + F_QRD, 1u);
      const u32 qb = lds0 + QSLOT + (aw >> 1) * 1152;
#pragma unroll
      for (int i = 0; i < 4; i++) {
        Q1[i] = lds_rd16(qb + i * STG + l * 16);
        const u32 q2 = lds_rd4(qb + i * STG + 1024 + (l & 31) * 4);
        Q2[i] = u32x4{(l >> 5) ? 0u : q2, 0u, 0u, 0u};
      }
      asm volatile("" : "+v"(Q1[0]), "+v"(Q1[1]), "+v"(Q1[2]), "+v"(Q1[3]), "+v"(Q2[0]), "+v"(Q2[1]), "+v"(Q2[2]), "+v"(Q2[3]));
      lds_flag_set(flags + F_QCN + 4 * aw, 1u);
    }
    STAMP(1, w, 4);
    if (aw == 0) {
      poll16(ws, KVF_OFF + (b % KVREP) * 1024 + l * 16, tag);
      lds_flag_set(flags + F_KVD, 1u);
      if (b == 0 && l == 0) st4c(rs, EPOCH_OFF, 0, tag);
    } else {
      lds_flag_wait1(flags + F_KVD, 1u);
    }
    STAMP(1, w, 5);
    __builtin_amdgcn_s_setprio(PRIO_ATT);
    attn_body<4>(ws, rs, lds0, aw, g, tg, tag, Q1, Q2);
    __builtin_amdgcn_s_setprio(0);
  }

  {
    const int tt = w >> 2, kq = w & 3;
    const int h = l >> 5, c31 = l & 31;
    const bool hasx = tt == (g >> 2);
    const int token = 64 * tg + 32 * tt + c31;
    const int col = 32 * (g + 8 * (kq >> 1)) + 16 * (kq & 1) + 4 * h;
    const int colx = 512 + 16 * (g & 3) + 4 * h;
    const u32x2 xr0 = *(const u32x2*)(x + (size_t)token * 576 + col), xr1 = *(const u32x2*)(x + (size_t)token * 576 + col + 8);
    u32x2 xx0 = {0, 0}, xx1 = {0, 0};
    if (hasx && kq == 0) { xx0 = *(const u32x2*)(x + (size_t)token * 576 + colx); xx1 = *(const u32x2*)(x + (size_t)token * 576 + colx + 8); }
    rawbar();
    STAMP(1, w, 13);
    u32x4 of[9];
#pragma unroll
    for (int i = 0; i < 8; i++) of[i] = ld16c(rs, l * 16, OF_OFF + (u32)((tg * 36 + 8 * kq + i) * 2 + tt) * 1024u);
    of[8] = ld16c(rs, l * 16, OF_OFF + (u32)((tg * 36 + 32 + kq) * 2 + tt) * 1024u);
    const int G1 = (l >> 4) & 1, q4 = (l & 15) >> 2, p4 = l & 3;
    const u32 cs = (u32)(G1 * 32 + 8 * p4) ^ (u32)(((q4 >> 1) & 1) * 64);
    const u32 aE = lds0 + (8 * kq * 18 + 4 * h + q4) * 128 + cs;
    const int rowx = 18 * (8 * kq + 4 * h + (q4 >> 1)) + 16 + (q4 & 1);
    const u32 aX = lds0 + rowx * 128 + cs;
    const u32 eE = lds0 + WOX0 + (8 * kq * 18 + 4 * h + q4) * 32 + 8 * p4;
    const u32 eX = lds0 + WOX0 + rowx * 32 + 8 * p4;
    f32x16 sa[2] = {f32x16{}, f32x16{}}, sx = {};
#pragma unroll
    for (int i = 0; i < 8; i++) {
#pragma unroll
      for (int d = 0; d < 2; d++) {
        const u32 a = (aE ^ (64 * ((i + d) & 1))) + i * 2304;
        sa[d] = mfma32(tr2(a, a + 1024), of[i], sa[d]);
      }
      if (hasx) sx = mfma32(tr2(eE + i * 576, eE + i * 576 + 256), of[i], sx);
    }
#pragma unroll
    for (int d = 0; d < 2; d++) {
      const u32 a = aX ^ (64 * d);
      sa[d] = mfma32(tr2(a, a + 36 * 128), of[8], sa[d]);
    }
    if (hasx) sx = mfma32(tr2(eX, eX + 36 * 32), of[8], sx);
    const u32 rb = lds0 + RED0 + l * 16;
#pragma unroll
    for (int pi = 0; pi < 4; pi++) {
      if (pi != kq) {
        const f32x16& s = sa[pi >> 1];
        const int o = 8 * (pi & 1);
        const u32 a = rb + (u32)((tt * 5 + pi) * 3 + ((kq - pi - 1) & 3)) * 2048u;
        lds_wr16(a, __builtin_bit_cast(u32x4, f32x4{s[o], s[o + 1], s[o + 2], s[o + 3]}));
        lds_wr16(a + 1024, __builtin_bit_cast(u32x4, f32x4{s[o + 4], s[o + 5], s[o + 6], s[o + 7]}));
      }
    }
    if (hasx && kq != 0) {
      const u32 a = rb + (u32)((tt * 5 + 4) * 3 + (kq - 1)) * 2048u;
      lds_wr16(a, __builtin_bit_cast(u32x4, f32x4{sx[0], sx[1], sx[2], sx[3]}));
      lds_wr16(a + 1024, __builtin_bit_cast(u32x4, f32x4{sx[4], sx[5], sx[6], sx[7]}));
    }
    rawbar();
    auto finish = [&](float (&s)[8], int pi, int gitem, u32x2 xa, u32x2 xb2, int c) {
#pragma unroll
      for (int j = 0; j < 3; j++) {
        const u32 a = rb + (u32)((tt * 5 + pi) * 3 + j) * 2048u;
        const f32x4 p0 = __builtin_bit_cast(f32x4, lds_rd16(a)), p1 = __builtin_bit_cast(f32x4, lds_rd16(a + 1024));
#pragma unroll
        for (int r = 0; r < 4; r++) { s[r] += p0[r]; s[4 + r] += p1[r]; }
      }
      const u32x4 gv = lds_rd16(lds0 + GG0 + gitem * 1024 + l * 16);
      const u32 g0 = gv[0], g1 = gv[1], g2 = gv[2], g3 = gv[3];
      const u32 x0 = xa[0], x1 = xa[1], x2 = xb2[0], x3 = xb2[1];
      u32x2 o0, o1;
      o0[0] = cvtpk(lo2f(x0) + rbf(rbf(s[0]) * lo2f(g0)), hi2f(x0) + rbf(rbf(s[1]) * hi2f(g0)));
      o0[1] = cvtpk(lo2f(x1) + rbf(rbf(s[2]) * lo2f(g1)), hi2f(x1) + rbf(rbf(s[3]) * hi2f(g1)));
      o1[0] = cvtpk(lo2f(x2) + rbf(rbf(s[4]) * lo2f(g2)), hi2f(x2) + rbf(rbf(s[5]) * hi2f(g2)));
      o1[1] = cvtpk(lo2f(x3) + rbf(rbf(s[6]) * lo2f(g3)), hi2f(x3) + rbf(rbf(s[7]) * hi2f(g3)));
      *(u32x2*)(out + (size_t)token * 576 + c) = o0;
      *(u32x2*)(out + (size_t)token * 576 + c + 8) = o1;
    };
    {
      float s[8];
#pragma unroll
      for (int r = 0; r < 8; r++) {
        const float v0 = (kq & 1) ? sa[0][8 + r] : sa[0][r], v1 = (kq & 1) ? sa[1][8 + r] : sa[1][r];
        s[r] = (kq >> 1) ? v1 : v0;
      }
      finish(s, kq, tt * 4 + kq, xr0, xr1, col);
    }
    if (hasx && kq == 0) {
      float s[8];
#pragma unroll
      for (int r = 0; r < 8; r++) s[r] = sx[r];
      finish(s, 4, 8, xx0, xx1, colx);
    }
    STAMP(1, w, 14);
  }
}

extern "C" void kernel_launch(void* const* d_in, const int* in_sizes, int n_in, void* d_out, int out_size, void* d_ws,
                              size_t ws_size, hipStream_t stream) {
  const u16* x = (const u16*)d_in[0];
  const u16* wq = (const u16*)d_in[1];
  const u16* wk = (const u16*)d_in[2];
  const u16* wv = (const u16*)d_in[3];
  const u16* wo = (const u16*)d_in[4];
  const u16* w1 = (const u16*)d_in[6];
  const u16* w3 = (const u16*)d_in[7];
  const u16* w2 = (const u16*)d_in[8];
  char* ws = (char*)d_ws;
  (void)hipFuncSetAttribute((const void*)fused_kernel, hipFuncAttributeMaxDynamicSharedMemorySize, LDS_TOTAL);
  fused_kernel<<<256, 512, LDS_TOTAL, stream>>>(x, wq, wk, wv, wo, w1, w3, w2, ws, (u16*)d_out);
}
```

```cpp
#include <hip/hip_runtime.h>
#include <type_traits>
#include <stdint.h>

typedef unsigned short u16;
typedef unsigned int u32;
typedef __attribute__((ext_vector_type(8))) __bf16 bf16x8;
typedef __attribute__((ext_vector_type(2))) __bf16 bf16x2;
typedef __attribute__((ext_vector_type(4))) short s16x4;
typedef __attribute__((ext_vector_type(16))) float f32x16;
typedef __attribute__((ext_vector_type(4))) float f32x4;
typedef __attribute__((ext_vector_type(2))) float f32x2;
typedef __attribute__((ext_vector_type(4))) u32 u32x4;
typedef __attribute__((ext_vector_type(2))) u32 u32x2;
typedef __amdgpu_buffer_rsrc_t rsrc_t;
#define LDS_AS __attribute__((address_space(3)))
#define SCHED_FENCE() __builtin_amdgcn_sched_barrier(0)

constexpr u32 EPOCH_OFF = 0;
constexpr int KVREP = 16;
constexpr u32 KVF_OFF = 4096;
constexpr u32 PFL_OFF = KVF_OFF + KVREP * 1024;
constexpr u32 OFL_OFF = PFL_OFF + 4096;
constexpr u32 K1F_OFF = OFL_OFF + 4096;
constexpr u32 K2_OFF = K1F_OFF + 65536;
constexpr u32 VF_OFF = K2_OFF + 8192;
constexpr u32 OF_OFF = VF_OFF + 131072;
constexpr u32 PART_OFF = OF_OFF + 2359296;
constexpr u32 WS_END = PART_OFF + 18874368;

constexpr u32 RING0 = 73728;
constexpr u32 STG = 5120;
constexpr u32 SLOT = 4 * STG;
constexpr u32 QSLOT = RING0 + 3 * SLOT;
constexpr u32 KVRED = 156672;
constexpr u32 FLAGS = 162816;
constexpr u32 LDS_TOTAL = 163840;
constexpr u32 XCH0 = 110592;
constexpr u32 WOX0 = 73728;
constexpr u32 RED0 = 92160;
constexpr u32 GG0 = 153600;
constexpr u32 F_BAR = 0, F_KVP = 16, F_QRD = 32, F_QCN = 48, F_KVD = 64, F_PX = 80, F_OCNT = 96, F_PCNT = 100, F_PRD = 104;
constexpr u32 PH_QEND = 4, PH_XDEAD = 5, PH_HRDY = 6, PH_DONE = 7;

__device__ __forceinline__ u32 cvtpk(float a, float b) {
  f32x2 v = {a, b};
  bf16x2 r = __builtin_convertvector(v, bf16x2);
  return __builtin_bit_cast(u32, r);
}
__device__ __forceinline__ float dot2bf(u32 a, u32 b, float c) {
  return __builtin_amdgcn_fdot2_f32_bf16(__builtin_bit_cast(bf16x2, a), __builtin_bit_cast(bf16x2, b), c, false);
}
__device__ __forceinline__ void unpack_add8(u32 u0, u32 u1, u32 u2, u32 u3, u32 sl, u32 sh, float c, float (&d)[8]) {
  asm("v_dot2_f32_bf16 %0, %8, %12, %14\n\tv_dot2_f32_bf16 %1, %8, %13, %14\n\t"
      "v_dot2_f32_bf16 %2, %9, %12, %14\n\tv_dot2_f32_bf16 %3, %9, %13, %14\n\t"
      "v_dot2_f32_bf16 %4, %10, %12, %14\n\tv_dot2_f32_bf16 %5, %10, %13, %14\n\t"
      "v_dot2_f32_bf16 %6, %11, %12, %14\n\tv_dot2_f32_bf16 %7, %11, %13, %14\n\ts_nop 2"
      : "=&v"(d[0]), "=&v"(d[1]), "=&v"(d[2]), "=&v"(d[3]), "=&v"(d[4]), "=&v"(d[5]), "=&v"(d[6]), "=&v"(d[7])
      : "v"(u0), "v"(u1), "v"(u2), "v"(u3), "v"(sl), "v"(sh), "v"(c));
}
__device__ __forceinline__ float lo2f(u32 v) { return __uint_as_float(v << 16); }
__device__ __forceinline__ float hi2f(u32 v) { return __uint_as_float(v & 0xffff0000u); }
__device__ __forceinline__ float rbf(float x) { return lo2f(cvtpk(x, x)); }
__device__ __forceinline__ f32x16 mfma32(u32x4 a, u32x4 b, f32x16 c) {
  return __builtin_amdgcn_mfma_f32_32x32x16_bf16(__builtin_bit_cast(bf16x8, a), __builtin_bit_cast(bf16x8, b), c, 0, 0, 0);
}
__device__ __forceinline__ f32x4 mfma16k16(u32x2 a, u32x2 b, f32x4 c) {
  return __builtin_amdgcn_mfma_f32_16x16x16bf16_1k(__builtin_bit_cast(s16x4, a), __builtin_bit_cast(s16x4, b), c, 0, 0, 0);
}
__device__ __forceinline__ u32x4 tr2(u32 addr0, u32 addr1) {
  s16x4 a = __builtin_amdgcn_ds_read_tr16_b64_v4i16((LDS_AS s16x4*)(uintptr_t)addr0);
  s16x4 b = __builtin_amdgcn_ds_read_tr16_b64_v4i16((LDS_AS s16x4*)(uintptr_t)addr1);
  u32x2 x = __builtin_bit_cast(u32x2, a), y = __builtin_bit_cast(u32x2, b);
  return u32x4{x[0], x[1], y[0], y[1]};
}
__device__ __forceinline__ u32x4 lds_rd16(u32 addr) { return *(LDS_AS u32x4*)(uintptr_t)addr; }
__device__ __forceinline__ u32x2 lds_rd8(u32 addr) { return *(LDS_AS u32x2*)(uintptr_t)addr; }
__device__ __forceinline__ u32 lds_rd4(u32 addr) { return *(LDS_AS u32*)(uintptr_t)addr; }
__device__ __forceinline__ void lds_wr16(u32 addr, u32x4 v) { *(LDS_AS u32x4*)(uintptr_t)addr = v; }
__device__ __forceinline__ void lds_wr4(u32 addr, u32 v) { *(LDS_AS u32*)(uintptr_t)addr = v; }
template <int N>
__device__ __forceinline__ void waitvm() { asm volatile("s_waitcnt vmcnt(%0)" ::"n"(N) : "memory"); }
__device__ __forceinline__ void waitlgkm0() { asm volatile("s_waitcnt lgkmcnt(0)" ::: "memory"); }
__device__ __forceinline__ void rawbar() { asm volatile("s_waitcnt lgkmcnt(0)\n\ts_barrier" ::: "memory"); }
__device__ __forceinline__ void glds(u32 m0val, u32 voff, const void* sbase) {
  asm volatile("s_mov_b32 m0, %0\n\ts_nop 0\n\tglobal_load_lds_dwordx4 %1, %2" ::"s"(m0val), "v"(voff), "s"(sbase) : "memory");
}
__device__ __forceinline__ float silu_f(float g) {
  return g * __builtin_amdgcn_rcpf(1.0f + __builtin_amdgcn_exp2f(g * -1.44269504088896f));
}
__device__ __forceinline__ u32x4 ld16c(rsrc_t r, u32 voff, u32 soff) {
  return __builtin_bit_cast(u32x4, __builtin_amdgcn_raw_buffer_load_b128(r, voff, soff, 16));
}
__device__ __forceinline__ u32 ld4c(rsrc_t r, u32 voff, u32 soff) { return __builtin_amdgcn_raw_buffer_load_b32(r, voff, soff, 16); }
__device__ __forceinline__ void st16c(rsrc_t r, u32 voff, u32 soff, u32x4 v) { __builtin_amdgcn_raw_buffer_store_b128(v, r, voff, soff, 16); }
__device__ __forceinline__ void st8c(rsrc_t r, u32 voff, u32 soff, u32x2 v) { __builtin_amdgcn_raw_buffer_store_b64(v, r, voff, soff, 16); }
__device__ __forceinline__ void st4c(rsrc_t r, u32 voff, u32 soff, u32 v) { __builtin_amdgcn_raw_buffer_store_b32(v, r, voff, soff, 16); }

#define SPIN_MAX 200000
__device__ __forceinline__ void lds_flag_set(u32 addr, u32 val) {
  asm volatile("s_waitcnt lgkmcnt(0)\n\tds_write_b32 %0, %1" ::"v"(addr), "v"(val) : "memory");
}
#define LDS_LOOP for (;;)
template <int SL = 1>
__device__ __forceinline__ void lds_flag_wait1(u32 addr, u32 val) {
  LDS_LOOP {
    u32 f;
    asm volatile("ds_read_b32 %0, %1\n\ts_waitcnt lgkmcnt(0)" : "=v"(f) : "v"(addr) : "memory");
    if (__builtin_amdgcn_readfirstlane(f) >= val) break;
    __builtin_amdgcn_s_sleep(SL);
  }
}
__device__ __forceinline__ void lds_flag_wait4(u32 base, u32 val) {
  LDS_LOOP {
    u32x4 f;
    asm volatile("ds_read_b128 %0, %1\n\ts_waitcnt lgkmcnt(0)" : "=v"(f) : "v"(base) : "memory");
    u32 m = min(min(f[0], f[1]), min(f[2], f[3]));
    if (__builtin_amdgcn_readfirstlane(m) >= val) break;
    __builtin_amdgcn_s_sleep(1);
  }
}
__device__ __forceinline__ void lds_bar4(u32 base, int w, u32 phase) {
  lds_flag_set(base + 4 * w, phase);
  lds_flag_wait4(base, phase);
}
__device__ __forceinline__ u32 lds_arrive(u32 addr) {
  u32 old = 0;
  if ((threadIdx.x & 63) == 0) {
    u32 one = 1u;
    asm volatile("s_waitcnt lgkmcnt(0)\n\tds_add_rtn_u32 %0, %1, %2\n\ts_waitcnt lgkmcnt(0)" : "=v"(old) : "v"(addr), "v"(one) : "memory");
  }
  return __builtin_amdgcn_readfirstlane(old);
}
template <int SL = 2>
__device__ __forceinline__ void poll4(const char* ws, u32 voff, u32 tag) {
  for (int it = 0; it < SPIN_MAX; it++) {
    u32 f;
    asm volatile("global_load_dword %0, %1, %2 sc1\n\ts_waitcnt vmcnt(0)" : "=v"(f) : "v"(voff), "s"(ws) : "memory");
    if (__all(f == tag)) break;
    __builtin_amdgcn_s_sleep(SL);
  }
}
__device__ __forceinline__ void poll16(const char* ws, u32 voff, u32 tag) {
  for (int it = 0; it < SPIN_MAX; it++) {
    u32x4 f;
    asm volatile("global_load_dwordx4 %0, %1, %2 sc1\n\ts_waitcnt vmcnt(0)" : "=v"(f) : "v"(voff), "s"(ws) : "memory");
    const u32 a = f[0], b = f[1], c = f[2], d = f[3];
    if (__all(a == tag && b == tag && c == tag && d == tag)) break;
    __builtin_amdgcn_s_sleep(2);
  }
}

#define STAMP(kid, wv, i)
constexpr float QSCALE = 0.23570226039551584f * 1.4426950408889634f;

template <int CTRL>
__device__ __forceinline__ float dpp_add(float x) {
  return x + __builtin_bit_cast(float, __builtin_amdgcn_mov_dpp(__builtin_bit_cast(int, x), CTRL, 0xf, 0xf, true));
}
__device__ __forceinline__ float red8(float x) {
  x = dpp_add<0xB1>(x);
  x = dpp_add<0x4E>(x);
  x = dpp_add<0x141>(x);
  return x;
}

__device__ __forceinline__ float max16(const f32x16& s) {
  float a = fmaxf(fmaxf(s[0], s[1]), s[2]), b = fmaxf(fmaxf(s[3], s[4]), s[5]);
  float c = fmaxf(fmaxf(s[6], s[7]), s[8]), d = fmaxf(fmaxf(s[9], s[10]), s[11]);
  float e = fmaxf(fmaxf(s[12], s[13]), s[14]);
  return fmaxf(fmaxf(fmaxf(a, b), fmaxf(c, d)), fmaxf(e, s[15]));
}
#define AGRP 0
#define ATT_TILES 32
#define FFN_GROUPS 9
template <int NB>
__device__ __forceinline__ void attn_body(char* __restrict__ ws, const rsrc_t rs, const u32 lds0, const int w, const int g,
                                          const int tg, const u32 tag, u32x4 (&Q1)[4], u32x4 (&Q2)[4]) {
  const int l = threadIdx.x & 63;
  const int h = l >> 5, c31 = l & 31;
  const int tt = w >> 1, hf = w & 1;
  const u32 flags = lds0 + FLAGS;
  const u32 k1v = K1F_OFF + l * 16;
  const u32 k2v = K2_OFF + c31 * 4;
  const u32 vv = VF_OFF + (h * 32 + (c31 < 18 ? c31 : 18)) * 16;
  const f32x16 zero16 = {};
  {
    u32x4 k1 = ld16c(rs, k1v, 0);
    u32x4 a2 = u32x4{ld4c(rs, k2v, 0), 0x3F803F80u, 0u, 0u};
#pragma unroll
    for (int i = 0; i < 4; i++) {
      f32x16 s = mfma32(k1, Q1[i], zero16);
      s = mfma32(a2, Q2[i], s);
      float m = max16(s);
      m = fmaxf(m, __shfl_xor(m, 32));
      float mr = m + 80.0f;
      float hi = rbf(mr), lo = rbf(mr - hi);
      Q2[i][1] = h ? 0u : cvtpk(-hi, -lo);
    }
  }
  STAMP(1, w + 4, 6);
  f32x16 O[4];
#pragma unroll
  for (int i = 0; i < 4; i++) O[i] = zero16;
  const int t0 = 32 * hf;
  u32x4 K1[NB], V0[NB], V1[NB];
  u32 K2[NB];
  auto loadK = [&](int bufi, int tile) {
    K1[bufi] = ld16c(rs, k1v, (u32)tile * 1024u);
    K2[bufi] = ld4c(rs, k2v, (u32)tile * 128u);
  };
  auto loadV = [&](int bufi, int tile) {
    V0[bufi] = ld16c(rs, vv, (u32)tile * 2048u);
    V1[bufi] = ld16c(rs, vv + 1024, (u32)tile * 2048u);
  };
#pragma unroll
  for (int j = 0; j < NB; j++) loadK(j, t0 + j);
#pragma unroll
  for (int j = 0; j < NB - 1; j++) loadV(j, t0 + j);
  f32x16 Sc = mfma32(K1[0], Q1[0], zero16);
  Sc = mfma32(u32x4{K2[0], 0x3F803F80u, 0u, 0u}, Q2[0], Sc);
  static_assert(NB == 4, "");
  u32x4 Pp = {0u, 0u, 0u, 0u};
  V1[NB - 1] = u32x4{0u, 0u, 0u, 0u};
  u32x4 A2[2] = {u32x4{K2[0], 0x3F803F80u, 0u, 0u}, u32x4{0u, 0x3F803F80u, 0u, 0u}};
  asm volatile("" : "+v"(A2[0]), "+v"(A2[1]));
  u32 sk1 = (u32)(t0 + NB) * 1024u, sk2 = (u32)(t0 + NB) * 128u, sv = (u32)(t0 + NB - 1) * 2048u;
#pragma unroll 1
  for (int n0 = 0; n0 < ATT_TILES; n0 += NB) {
#pragma unroll
    for (int u = 0; u < NB; u++) {
#pragma unroll
      for (int i = 0; i < 4; i++) {
        const int ni = (i + 1) & 3;
        const int nb = (i == 3) ? (u + 1) % NB : u;
        const int pi = (i + 3) & 3;
        const int pb = (i == 0) ? (u + NB - 1) % NB : u;
        f32x16 Sn = mfma32(K1[nb], Q1[ni], zero16);
        float e0 = __builtin_amdgcn_exp2f(Sc[0]), e1 = __builtin_amdgcn_exp2f(Sc[1]);
        float e2 = __builtin_amdgcn_exp2f(Sc[2]), e3 = __builtin_amdgcn_exp2f(Sc[3]);
        u32x4 P0, P1;
        P0[0] = cvtpk(e0, e1); P0[1] = cvtpk(e2, e3);
        SCHED_FENCE();
        Sn = mfma32(A2[nb & 1], Q2[ni], Sn);
        e0 = __builtin_amdgcn_exp2f(Sc[4]); e1 = __builtin_amdgcn_exp2f(Sc[5]);
        e2 = __builtin_amdgcn_exp2f(Sc[6]); e3 = __builtin_amdgcn_exp2f(Sc[7]);
        P0[2] = cvtpk(e0, e1); P0[3] = cvtpk(e2, e3);
        SCHED_FENCE();
        O[pi] = mfma32(V1[pb], Pp, O[pi]);
        if (i == 0) {
          V0[(u + NB - 1) % NB] = ld16c(rs, vv + (u & 1) * 2048, sv + (u >> 1) * 4096);
          V1[(u + NB - 1) % NB] = ld16c(rs, vv + (u & 1) * 2048 + 1024, sv + (u >> 1) * 4096);
        }
        if (i == 2) { A2[(u + 1) & 1][0] = K2[(u + 1) % NB]; asm volatile("" : "+v"(A2[(u + 1) & 1])); }
        if (i == 3) {
          K1[u] = ld16c(rs, k1v + u * 1024, sk1);
          K2[u] = ld4c(rs, k2v + u * 128, sk2);
        }
        e0 = __builtin_amdgcn_exp2f(Sc[8]); e1 = __builtin_amdgcn_exp2f(Sc[9]);
        e2 = __builtin_amdgcn_exp2f(Sc[10]); e3 = __builtin_amdgcn_exp2f(Sc[11]);
        P1[0] = cvtpk(e0, e1); P1[1] = cvtpk(e2, e3);
        SCHED_FENCE();
        O[i] = mfma32(V0[u], P0, O[i]);
        e0 = __builtin_amdgcn_exp2f(Sc[12]); e1 = __builtin_amdgcn_exp2f(Sc[13]);
        e2 = __builtin_amdgcn_exp2f(Sc[14]); e3 = __builtin_amdgcn_exp2f(Sc[15]);
        P1[2] = cvtpk(e0, e1); P1[3] = cvtpk(e2, e3);
        SCHED_FENCE();
        Pp = P1;
        Sc = Sn;
      }
    }
    sk1 += 4096u; sk2 += 512u; sv += 8192u;
  }
  O[3] = mfma32(V1[NB - 1], Pp, O[3]);
  STAMP(1, w + 4, 7);
  f32x16 Ka, Kb, Sa, Sb;
  if (hf == 0) { Ka = O[0]; Kb = O[1]; Sa = O[2]; Sb = O[3]; }
  else { Ka = O[2]; Kb = O[3]; Sa = O[0]; Sb = O[1]; }
  lds_flag_wait4(flags + F_BAR, PH_DONE);
  const u32 xa = lds0 + XCH0 + l * 16;
#pragma unroll
  for (int j = 0; j < 3; j++) {
    lds_wr16(xa + ((w * 2 + 0) * 3 + j) * 1024, __builtin_bit_cast(u32x4, f32x4{Sa[4 * j], Sa[4 * j + 1], Sa[4 * j + 2], Sa[4 * j + 3]}));
    lds_wr16(xa + ((w * 2 + 1) * 3 + j) * 1024, __builtin_bit_cast(u32x4, f32x4{Sb[4 * j], Sb[4 * j + 1], Sb[4 * j + 2], Sb[4 * j + 3]}));
  }
  const int pw = w ^ 1;
  lds_flag_set(flags + F_PX + 4 * w, 1u);
  lds_flag_wait1(flags + F_PX + 4 * pw, 1u);
#pragma unroll
  for (int j = 0; j < 3; j++) {
    f32x4 ra = __builtin_bit_cast(f32x4, lds_rd16(xa + ((pw * 2 + 0) * 3 + j) * 1024));
    f32x4 rb = __builtin_bit_cast(f32x4, lds_rd16(xa + ((pw * 2 + 1) * 3 + j) * 1024));
#pragma unroll
    for (int r = 0; r < 4; r++) { Ka[4 * j + r] += ra[r]; Kb[4 * j + r] += rb[r]; }
  }
  STAMP(1, w + 4, 8);
  {
    float z = 0.f;
#pragma unroll
    for (int r = 0; r < 12; r++) { z = fmaf(Ka[r], 0.f, z); z = fmaf(Kb[r], 0.f, z); }
    bool bad = !(z == 0.f) || !(Ka[10] >= 8e-28f) || !(Kb[10] >= 8e-28f);
    if (__any(bad)) {
      u32x4 q1a, q2a, q1b, q2b;
      if (hf == 0) { q1a = Q1[0]; q2a = Q2[0]; q1b = Q1[1]; q2b = Q2[1]; }
      else { q1a = Q1[2]; q2a = Q2[2]; q1b = Q1[3]; q2b = Q2[3]; }
      q2a[1] = 0u; q2b[1] = 0u;
      float ma = -INFINITY, mb = -INFINITY;
#pragma unroll 1
      for (int t = 0; t < 64; t++) {
        u32x4 k1 = ld16c(rs, k1v, (u32)t * 1024u);
        u32x4 a2 = u32x4{ld4c(rs, k2v, (u32)t * 128u), 0x3F803F80u, 0u, 0u};
        f32x16 s = mfma32(k1, q1a, zero16); s = mfma32(a2, q2a, s);
        ma = fmaxf(ma, max16(s));
        s = mfma32(k1, q1b, zero16); s = mfma32(a2, q2b, s);
        mb = fmaxf(mb, max16(s));
      }
      ma = fmaxf(ma, __shfl_xor(ma, 32));
      mb = fmaxf(mb, __shfl_xor(mb, 32));
      Ka = zero16; Kb = zero16;
#pragma unroll 1
      for (int t = 0; t < 64; t++) {
        u32x4 k1 = ld16c(rs, k1v, (u32)t * 1024u);
        u32x4 a2 = u32x4{ld4c(rs, k2v, (u32)t * 128u), 0x3F803F80u, 0u, 0u};
        u32x4 v0 = ld16c(rs, vv, (u32)t * 2048u), v1 = ld16c(rs, vv + 1024, (u32)t * 2048u);
        f32x16 s = mfma32(k1, q1a, zero16); s = mfma32(a2, q2a, s);
        u32x4 p0, p1;
#pragma unroll
        for (int r = 0; r < 4; r++) {
          p0[r] = cvtpk(__builtin_amdgcn_exp2f(s[2 * r] - ma), __builtin_amdgcn_exp2f(s[2 * r + 1] - ma));
          p1[r] = cvtpk(__builtin_amdgcn_exp2f(s[8 + 2 * r] - ma), __builtin_amdgcn_exp2f(s[9 + 2 * r] - ma));
        }
        Ka = mfma32(v0, p0, Ka); Ka = mfma32(v1, p1, Ka);
        s = mfma32(k1, q1b, zero16); s = mfma32(a2, q2b, s);
#pragma unroll
        for (int r = 0; r < 4; r++) {
          p0[r] = cvtpk(__builtin_amdgcn_exp2f(s[2 * r] - mb), __builtin_amdgcn_exp2f(s[2 * r + 1] - mb));
          p1[r] = cvtpk(__builtin_amdgcn_exp2f(s[8 + 2 * r] - mb), __builtin_amdgcn_exp2f(s[9 + 2 * r] - mb));
        }
        Kb = mfma32(v0, p0, Kb); Kb = mfma32(v1, p1, Kb);
      }
    }
  }
  {
    const float ia = 1.0f / Ka[10], ib = 1.0f / Kb[10];
    u32x4 oa, ob;
#pragma unroll
    for (int r = 0; r < 4; r++) {
      oa[r] = cvtpk(Ka[2 * r] * ia, Ka[2 * r + 1] * ia);
      ob[r] = cvtpk(Kb[2 * r] * ib, Kb[2 * r + 1] * ib);
    }
    const u32 so = OF_OFF + (u32)((tg * 36 + 4 * g + 2 * hf) * 2 + tt) * 1024u;
    st16c(rs, l * 16, so, oa);
    st16c(rs, l * 16, so + 2048, ob);
    if (h == 0) {
      u32x2 ex = {cvtpk(Ka[8] * ia, Ka[9] * ia), cvtpk(Kb[8] * ib, Kb[9] * ib)};
      st8c(rs, ((g & 1) * 32 + c31) * 16 + 8 * hf, OF_OFF + (u32)((tg * 36 + 32 + (g >> 1)) * 2 + tt) * 1024u, ex);
    }
    waitvm<0>();
    if (lds_arrive(flags + F_OCNT) == 3) {
      if (l == 0) st4c(rs, OFL_OFF + g * 4, (u32)tg * 128u, tag);
    }
  }
  STAMP(1, w + 4, 9);
}

#define GWX g
struct FfnLd {
  const char* s0;
  const char* s1;
  const char* sx;
  u32 vA, vB, vX;
};

__device__ __forceinline__ void glds_first(u32 m0val, u32 voff, const char* sbase) {
  asm volatile("s_mov_b32 m0, %0\n\ts_nop 0\n\tglobal_load_lds_dwordx4 %1, %2 offset:-2048" ::"s"(m0val), "v"(voff), "s"(sbase) : "memory");
}
template <int OFF>
__device__ __forceinline__ void glds_next(u32 voff, const char* sbase) {
  if constexpr (OFF == -1024) asm volatile("global_load_lds_dwordx4 %0, %1 offset:-1024" ::"v"(voff), "s"(sbase) : "memory");
  else if constexpr (OFF == 0) asm volatile("global_load_lds_dwordx4 %0, %1" ::"v"(voff), "s"(sbase) : "memory");
  else if constexpr (OFF == 1024) asm volatile("global_load_lds_dwordx4 %0, %1 offset:1024" ::"v"(voff), "s"(sbase) : "memory");
  else asm volatile("global_load_lds_dwordx4 %0, %1 offset:2048" ::"v"(voff), "s"(sbase) : "memory");
}
__device__ __forceinline__ void ffn_issue_stage(u32 m0val, const FfnLd& ld) {
  glds_first(m0val, ld.vA, ld.s0);
  glds_next<-1024>(ld.vB, ld.s0);
  glds_next<0>(ld.vA, ld.s1);
  glds_next<1024>(ld.vB, ld.s1);
  glds_next<2048>(ld.vX, ld.sx);
}

#define YMASK 0
#define PSTAUX 16
#define TPRIO 0
#define TPRIO1 TPRIO
#define TPRIO2 TPRIO1
#define TPRIO3 TPRIO2
#define ZPEEL 0
#define ZMOV 1
typedef double f64x8 __attribute__((ext_vector_type(8)));
__device__ __forceinline__ f32x16 zero16v() {
  double z0, z1, z2, z3, z4, z5, z6, z7;
  asm volatile("v_mov_b64 %0, 0\n\tv_mov_b64 %1, 0\n\tv_mov_b64 %2, 0\n\tv_mov_b64 %3, 0\n\t"
               "v_mov_b64 %4, 0\n\tv_mov_b64 %5, 0\n\tv_mov_b64 %6, 0\n\tv_mov_b64 %7, 0"
               : "=v"(z0), "=v"(z1), "=v"(z2), "=v"(z3), "=v"(z4), "=v"(z5), "=v"(z6), "=v"(z7));
  const f64x8 v = {z0, z1, z2, z3, z4, z5, z6, z7};
  return __builtin_bit_cast(f32x16, v);
}
#define PSL0 8
#define PSL1 16
#define OSL 4
#define PRIO_FFN 3
#define PRIO_Q 2
#define PRIO_KV 3
#define FHI 2
template <int I>
__device__ __forceinline__ void fprio() {
  if constexpr (YMASK != 0) {
    constexpr bool lo = (YMASK >> I) & 1;
    constexpr bool plo = (YMASK >> ((I + 8) % 9)) & 1;
    if constexpr (lo != plo) { __builtin_amdgcn_s_setprio(lo ? 0 : FHI); __builtin_amdgcn_sched_barrier(0); }
  }
}
template <int SL, int WAITN, bool ZERO = false>
__device__ __forceinline__ void ffn_step(f32x16 (&acc)[8], f32x16& accx, u32x4 (&A)[5], u32x4 (&Bc)[2], u32 aAddr0,
                                         u32 aAddr1, u32 xAddr0, u32 xAddr1, u32 bAddr0, u32 bAddr1, u32 ringm0,
                                         FfnLd& ld, u32 kstep) {
  constexpr u32 NS = ((SL + 1) & 3) * SLOT;
  waitvm<WAITN>();
  SCHED_FENCE();
  fprio<0>();
  acc[0] = mfma32(A[0], Bc[0], ZERO ? f32x16{} : acc[0]);
  u32x4 Bn0 = lds_rd16(bAddr0), Bn1 = lds_rd16(bAddr1);
  SCHED_FENCE();
  fprio<1>();
  acc[1] = mfma32(A[0], Bc[1], ZERO ? f32x16{} : acc[1]);
  A[0] = tr2(aAddr0 + NS, aAddr1 + NS);
  SCHED_FENCE();
  fprio<2>();
  acc[2] = mfma32(A[1], Bc[0], ZERO ? f32x16{} : acc[2]);
  SCHED_FENCE();
  fprio<3>();
  acc[3] = mfma32(A[1], Bc[1], ZERO ? f32x16{} : acc[3]);
  A[1] = tr2((aAddr0 ^ 64) + NS, (aAddr1 ^ 64) + NS);
  SCHED_FENCE();
  fprio<4>();
  acc[4] = mfma32(A[2], Bc[0], ZERO ? f32x16{} : acc[4]);
  glds_first(ringm0 + SL * SLOT, ld.vA, ld.s0);
  SCHED_FENCE();
  fprio<5>();
  acc[5] = mfma32(A[2], Bc[1], ZERO ? f32x16{} : acc[5]);
  A[2] = tr2(aAddr0 + NS + 2048, aAddr1 + NS + 2048);
  glds_next<-1024>(ld.vB, ld.s0);
  SCHED_FENCE();
  fprio<6>();
  acc[6] = mfma32(A[3], Bc[0], ZERO ? f32x16{} : acc[6]);
  SCHED_FENCE();
  fprio<7>();
  acc[7] = mfma32(A[3], Bc[1], ZERO ? f32x16{} : acc[7]);
  A[3] = tr2((aAddr0 ^ 64) + NS + 2048, (aAddr1 ^ 64) + NS + 2048);
  glds_next<0>(ld.vA, ld.s1);
  SCHED_FENCE();
  fprio<8>();
  accx = mfma32(A[4], Bc[0], ZERO ? f32x16{} : accx);
  A[4] = tr2(xAddr0 + NS, xAddr1 + NS);
  glds_next<1024>(ld.vB, ld.s1);
  glds_next<2048>(ld.vX, ld.sx);
  ld.vA += kstep; ld.vB += kstep; ld.vX += kstep;
  SCHED_FENCE();
  Bc[0] = Bn0; Bc[1] = Bn1;
}

__device__ __forceinline__ void ffn_body(const u16* __restrict__ wo, const u16* __restrict__ w1, const u16* __restrict__ w3,
                                         const u16* __restrict__ w2, char* __restrict__ ws, const rsrc_t rs, const u32 lds0,
                                         const int w, const int g, const int tg, const int rot, const u32 tag) {
  int l = threadIdx.x & 63;
  const int tl0 = w >> 1;
  const u32 flags = lds0 + FLAGS;
  const u32 ring = lds0 + RING0 + w * STG;
  const u32 ringm0 = ring + 2048;

  FfnLd ld;
  auto set_lane_offsets = [&](u32 strd, int kst) {
    int lo = l;
    asm volatile("" : "+v"(lo));
    const int r = lo >> 3, c = (lo & 7) ^ (((r >> 1) & 1) << 2);
    const u32 koff = (u32)kst * 16u * strd;
    ld.vA = r * strd + c * 16 + koff;
    ld.vB = ld.vA + 8 * strd - 1024;
    ld.vX = (lo >> 2) * strd + (lo & 3) * 16 + koff;
  };
  auto set_bases = [&](int pass) {
    if (pass < 2) {
      const size_t co = (size_t)GWX * 1152 + (size_t)(2 * w + pass) * 128;
      ld.s0 = (const char*)w1 + co + 2048;
      ld.s1 = (const char*)w3 + co;
      ld.sx = (const char*)(pass == 0 ? w1 : w3) + (size_t)GWX * 1152 + 1024 + (w & 1) * 64 - 2048;
    } else {
      const char* base = (const char*)w2 + (size_t)GWX * 576 * 1152;
      ld.s0 = base + w * 256 + 2048;
      ld.s1 = base + w * 256 + 128;
      ld.sx = base + 1024 + (w & 1) * 64 - 2048;
    }
  };
  set_bases(0);
  set_lane_offsets(9216, rot);
  auto issue_slot = [&](int s) {
    ffn_issue_stage(ringm0 + s * SLOT, ld);
    ld.vA += 16 * 9216; ld.vB += 16 * 9216; ld.vX += 16 * 9216;
  };
  {
    u32x4 q1[2];
    u32 q2[2];
    const int h = l >> 5, G1 = (l >> 4) & 1, q4 = (l & 15) >> 2, p4 = l & 3;
    const int s = 4 * G1 + p4;
    const u32 offE = (u32)(0x2020202018081000ULL >> (8 * s)) & 0xffu;
    const u32 offO = (u32)(0x2020201018081000ULL >> (8 * s)) & 0xffu;
    const u32 qa = lds0 + RING0 + (8 * h + q4) * 144 + ((w & 1) ? 36 * w - 4 + offO : 36 * w + offE);
    const u32 xb = lds0 + h * 512 + (((l & 31) ^ h) * 16);
    const u32 myflag = flags + F_BAR + 4 * w;
    f32x16 qacc[2] = {f32x16{}, f32x16{}};
    u32x4 Aq[3], B0[3], B1[3], fl = {0, 0, 0, 0};
    auto ldq = [&](int ks) {
      Aq[ks % 3] = tr2(qa + ks * 2304, qa + ks * 2304 + 576);
      const u32 xa = (xb ^ (32 * (ks & 3))) + ks * 2048;
      B0[ks % 3] = lds_rd16(xa);
      B1[ks % 3] = lds_rd16(xa + 1024);
    };
    ldq(0); ldq(1);
#pragma unroll
    for (int ks = 0; ks < 36; ks++) {
      if (ks + 2 < 36) ldq(ks + 2);
      if (ks % 9 == 1 && ks > 9) fl = *(volatile LDS_AS u32x4*)(uintptr_t)(flags + F_BAR);
      SCHED_FENCE();
      qacc[0] = mfma32(Aq[ks % 3], B0[ks % 3], qacc[0]);
      qacc[1] = mfma32(Aq[ks % 3], B1[ks % 3], qacc[1]);
      SCHED_FENCE();
      if (ks % 9 == 8 && ks < 35) {
        const u32 ph = (u32)(ks / 9 + 1);
        asm volatile("ds_write_b32 %0, %1" ::"v"(myflag), "v"(ph) : "memory");
      }
      if (ks % 9 == 2 && ks > 9) {
        const u32 ph = (u32)(ks / 9);
        const u32 m = min(min(fl[0], fl[1]), min(fl[2], fl[3]));
        if (__builtin_amdgcn_readfirstlane(m) < ph) lds_flag_wait4(flags + F_BAR, ph);
        issue_slot(ks / 9 - 1);
      }
      SCHED_FENCE();
    }
#pragma unroll
    for (int t = 0; t < 2; t++) {
      float v[10];
      if (w & 1) {
#pragma unroll
        for (int r = 0; r < 8; r++) v[r] = qacc[t][2 + r];
        v[8] = qacc[t][14]; v[9] = qacc[t][15];
      } else {
#pragma unroll
        for (int r = 0; r < 10; r++) v[r] = qacc[t][r];
      }
#pragma unroll
      for (int r = 0; r < 4; r++) q1[t][r] = cvtpk(rbf(v[2 * r]) * QSCALE, rbf(v[2 * r + 1]) * QSCALE);
      q2[t] = cvtpk(rbf(v[8]) * QSCALE, rbf(v[9]) * QSCALE);
    }
    lds_bar4(flags + F_BAR, w, PH_QEND);
    const u32 qb = lds0 + QSLOT + w * STG;
    lds_wr16(qb + l * 16, q1[0]);
    lds_wr16(qb + 1152 + l * 16, q1[1]);
    if (l < 32) { lds_wr4(qb + 1024 + l * 4, q2[0]); lds_wr4(qb + 1152 + 1024 + l * 4, q2[1]); }
    lds_flag_set(flags + F_QRD + 4 * w, 1u);
  }
  STAMP(1, w, 3);
  if (PRIO_Q != PRIO_FFN) __builtin_amdgcn_s_setprio(PRIO_FFN);

  f32x16 acc[8], accx;
  u32x4 GX[2];
  u32x4 A[5], Bc[2];
  u32x4 H0[4][2];
  const f32x16 zero16 = {};
#pragma unroll
  for (int t = 0; t < 4; t++) { H0[t][0] = u32x4{0, 0, 0, 0}; H0[t][1] = u32x4{0, 0, 0, 0}; }
  GX[0] = u32x4{0, 0, 0, 0}; GX[1] = u32x4{0, 0, 0, 0};

#pragma unroll 1
  for (int pass = 0; pass < 3; pass++) {
    u32 a0, a1, x0, x1, bL0, bL1;
    {
      int lo = l;
      asm volatile("" : "+v"(lo));
      const int h = lo >> 5, G1 = (lo >> 4) & 1, q4 = (lo & 15) >> 2, p4 = lo & 3;
      const u32 swz = ((q4 >> 1) & 1) * 64;
      const int hm = pass < 2 ? 8 : 4, tm = 12 - hm;
      a0 = ring + (hm * h + q4) * 128 + ((G1 * 32 + 8 * p4) ^ swz); a1 = a0 + tm * 128;
      x0 = ring + 4096 + (hm * h + q4) * 64 + G1 * 32 + 8 * p4; x1 = x0 + tm * 64;
      const u32 bl = lds0 + h * 512 + (((lo & 31) ^ h) * 16);
      bL0 = bl + tl0 * 1024; bL1 = bl + (1 - tl0) * 1024;
    }
#pragma unroll
    for (int i = 0; i < 8; i++) acc[i] = zero16v();
    accx = zero16v();
    Bc[0] = lds_rd16(bL0 + rot * 2048);
    Bc[1] = lds_rd16(bL1 + rot * 2048);
    waitvm<10>();
    SCHED_FENCE();
    A[0] = tr2(a0, a1);
    SCHED_FENCE();
    A[1] = tr2(a0 ^ 64, a1 ^ 64);
    SCHED_FENCE();
    A[2] = tr2(a0 + 2048, a1 + 2048);
    SCHED_FENCE();
    A[3] = tr2((a0 ^ 64) + 2048, (a1 ^ 64) + 2048);
    SCHED_FENCE();
    A[4] = tr2(x0, x1);
    SCHED_FENCE();
    if (pass == 0) {
      lds_flag_wait4(flags + F_QCN, 1u);
      issue_slot(3);
      STAMP(1, w, 4);
    }
    const u32 strd = pass < 2 ? 9216u : 1152u;
    u32 kstep = 16u * strd;
    int kb = rot;
    STAMP(1, w, 10 + pass);
#pragma unroll 1
    for (int j = 9 - FFN_GROUPS; j < 9; j++) {
      int kn = kb + 4;
      if (kn == 36) kn = 0;
      if (j == 8) {
        if (pass < 2) {
          const u32 st2 = pass == 0 ? 9216u : 1152u;
          set_bases(pass + 1); set_lane_offsets(st2, rot); kstep = 16u * st2;
        } else {
          ld.vA = 0; ld.vB = 0; ld.vX = 2048; kstep = 0;
        }
      } else if (kn == 0) {
        set_lane_offsets(strd, 0);
      }
      const u32 bb0 = bL0 + kb * 2048, bb1 = bL1 + kb * 2048;
      ffn_step<0, 10>(acc, accx, A, Bc, a0, a1, x0, x1, (bb0 ^ 32) + 2048, (bb1 ^ 32) + 2048, ringm0, ld, kstep);
      ffn_step<1, 10>(acc, accx, A, Bc, a0, a1, x0, x1, (bb0 ^ 64) + 4096, (bb1 ^ 64) + 4096, ringm0, ld, kstep);
      ffn_step<2, 10>(acc, accx, A, Bc, a0, a1, x0, x1, (bb0 ^ 96) + 6144, (bb1 ^ 96) + 6144, ringm0, ld, kstep);
      ffn_step<3, 10>(acc, accx, A, Bc, a0, a1, x0, x1, bL0 + kn * 2048, bL1 + kn * 2048, ringm0, ld, kstep);
      kb = kn;
    }
    if (pass == 0) {
#pragma unroll
      for (int t = 0; t < 4; t++)
#pragma unroll
        for (int s = 0; s < 2; s++) {
#pragma unroll
          for (int r = 0; r < 4; r++) {
            const int e = 8 * s + 2 * r;
            H0[t][s][r] = cvtpk(silu_f(acc[t][e]) * acc[4 + t][e], silu_f(acc[t][e + 1]) * acc[4 + t][e + 1]);
          }
          SCHED_FENCE();
        }
#pragma unroll
      for (int e = 0; e < 8; e++) GX[e >> 2][e & 3] = cvtpk(silu_f(accx[2 * e]), silu_f(accx[2 * e + 1]));
    } else if (pass == 1) {
      lds_bar4(flags + F_BAR, w, PH_XDEAD);
#pragma unroll
      for (int t = 0; t < 4; t++)
#pragma unroll
        for (int s = 0; s < 2; s++) {
          const int ft = t >> 1, ltt = t & 1;
          lds_wr16(((ltt == 0 ? bL0 : bL1) ^ (32 * ((2 * ft + s) & 3))) + (8 * w + 2 * ft + s) * 2048, H0[t][s]);
        }
      SCHED_FENCE();
#pragma unroll
      for (int t = 0; t < 4; t++)
#pragma unroll
        for (int s = 0; s < 2; s++) {
          u32x4 hh;
#pragma unroll
          for (int r = 0; r < 4; r++) {
            const int e = 8 * s + 2 * r;
            hh[r] = cvtpk(silu_f(acc[t][e]) * acc[4 + t][e], silu_f(acc[t][e + 1]) * acc[4 + t][e + 1]);
          }
          const int ft = t >> 1, ltt = t & 1;
          lds_wr16(((ltt == 0 ? bL0 : bL1) ^ (32 * ((2 * ft + s) & 3))) + (8 * w + 4 + 2 * ft + s) * 2048, hh);
          SCHED_FENCE();
        }
#pragma unroll
      for (int s = 0; s < 2; s++) {
        u32x4 hx;
#pragma unroll
        for (int r = 0; r < 4; r++) {
          const int e = 8 * s + 2 * r;
          hx[r] = cvtpk(lo2f(GX[s][r]) * accx[e], hi2f(GX[s][r]) * accx[e + 1]);
        }
        lds_wr16((bL0 ^ (32 * ((2 * (w & 1) + s) & 3))) + (32 + 2 * (w & 1) + s) * 2048, hx);
      }
      lds_bar4(flags + F_BAR, w, PH_HRDY);
    }
  }
  STAMP(1, w, 5);
  __builtin_amdgcn_s_setprio(TPRIO);
  waitvm<0>();
  lds_bar4(flags + F_BAR, w, PH_DONE);
  {
    int lo = l;
    asm volatile("" : "+v"(lo));
    const u32 so = PART_OFF + (u32)(g * 32 + tg) * 73728u;
#pragma unroll
    for (int t = 0; t < 8; t++) {
      const int dt = 4 * w + (t >> 1);
      const int tt = (t & 1) == 0 ? tl0 : 1 - tl0;
#pragma unroll
      for (int hh = 0; hh < 2; hh++) {
        u32x4 o;
#pragma unroll
        for (int r = 0; r < 4; r++) o[r] = cvtpk(acc[t][8 * hh + 2 * r], acc[t][8 * hh + 2 * r + 1]);
        __builtin_amdgcn_raw_buffer_store_b128(o, rs, lo * 16, so + (u32)((dt * 2 + tt) * 2 + hh) * 1024u, PSTAUX);
      }
    }
#pragma unroll
    for (int hh = 0; hh < 2; hh++) {
      u32x4 o;
#pragma unroll
      for (int r = 0; r < 4; r++) o[r] = cvtpk(accx[8 * hh + 2 * r], accx[8 * hh + 2 * r + 1]);
      __builtin_amdgcn_raw_buffer_store_b128(o, rs, lo * 16, so + (u32)(((16 + (w & 1)) * 2 + tl0) * 2 + hh) * 1024u, PSTAUX);
    }
    waitvm<0>();
    if (lds_arrive(flags + F_PCNT) == 3) {
      if (lo == 0) st4c(rs, PFL_OFF + g * 4, (u32)tg * 128u, tag);
    }
    STAMP(1, w, 6);
    if (TPRIO1 != TPRIO) __builtin_amdgcn_s_setprio(TPRIO1);
    {
      const int c = (lo & 7) ^ (((lo >> 4) & 1) << 2);
      u32 vo = (lo >> 3) * 1152 + (c >> 2 ? 64 * (g + 8) : 64 * g) + (c & 3) * 16 + w * 18 * 9216;
#pragma unroll 1
      for (int i = 0; i < 18; i++) { glds(lds0 + (w * 18 + i) * 1024, vo, wo); vo += 9216; }
      u32 ve = (lo >> 1) * 1152 + 1024 + 32 * (g & 3) + (lo & 1) * 16;
#pragma unroll 1
      for (int i = w; i < 18; i += 4) glds(lds0 + WOX0 + i * 1024, ve + i * 32 * 1152, wo);
    }
    if (w == 0) {
      poll4<PSL0>(ws, PFL_OFF + tg * 128 + (lo & 7) * 4, tag);
      lds_flag_set(flags + F_PRD, 1u);
    } else {
      lds_flag_wait1<PSL1>(flags + F_PRD, 1u);
    }
    if (TPRIO2 != TPRIO1) __builtin_amdgcn_s_setprio(TPRIO2);
    STAMP(1, w, 7);
    {
      const int tt = w >> 1, dt = g + 8 * (w & 1);
      const u32 sp = PART_OFF + (u32)tg * 73728u + (u32)((dt * 2 + tt) * 2) * 1024u;
      u32x4 P[2][8];
#pragma unroll
      for (int hh = 0; hh < 2; hh++)
#pragma unroll
        for (int gg = 0; gg < 8; gg++) P[hh][gg] = ld16c(rs, lo * 16, sp + (u32)hh * 1024u + (u32)gg * 32u * 73728u);
      u32x4 PX[8];
      if (w == 3) {
        const u32 sx = PART_OFF + (u32)tg * 73728u + (u32)(((16 + ((g & 3) >> 1)) * 2 + (g >> 2)) * 2 + (g & 1)) * 1024u;
#pragma unroll
        for (int gg = 0; gg < 8; gg++) PX[gg] = ld16c(rs, lo * 16, sx + (u32)gg * 32u * 73728u);
      }
      auto gate = [&](const u32x4(&p)[8]) {
        u32x4 o;
#pragma unroll
        for (int r = 0; r < 4; r++) {
          float f0 = 0.f, f1 = 0.f;
#pragma unroll
          for (int gg = 0; gg < 8; gg++) { f0 += lo2f(p[gg][r]); f1 += hi2f(p[gg][r]); }
          o[r] = cvtpk(silu_f(rbf(f0)), silu_f(rbf(f1)));
        }
        return o;
      };
#pragma unroll
      for (int hh = 0; hh < 2; hh++) lds_wr16(lds0 + GG0 + (tt * 4 + (w & 1) * 2 + hh) * 1024 + lo * 16, gate(P[hh]));
      if (w == 3) lds_wr16(lds0 + GG0 + 8 * 1024 + lo * 16, gate(PX));
    }
    STAMP(1, w, 8);
    waitvm<0>();
    if (TPRIO3 != TPRIO2) __builtin_amdgcn_s_setprio(TPRIO3);
    if (w == 0) poll4<OSL>(ws, OFL_OFF + tg * 128 + (lo & 7) * 4, tag);
    STAMP(1, w, 9);
  }
}

#define PRIO_ATT 1
#define XMAP 2
#define GLATE 2
#define WQ0 3
#define WQA 2
#define WQB1 2
#define WQC 3
#define LNDOT 1
#define PF 0
#define KVEARLY 0
#define GEARLY 1
#define XBAR
__global__ __launch_bounds__(512, 2) void fused_kernel(const u16* __restrict__ x, const u16* __restrict__ wq,
                                                       const u16* __restrict__ wk, const u16* __restrict__ wv,
                                                       const u16* __restrict__ wo, const u16* __restrict__ w1,
                                                       const u16* __restrict__ w3, const u16* __restrict__ w2,
                                                       char* __restrict__ ws, u16* __restrict__ out) {
  extern __shared__ __attribute__((aligned(1024))) char smem[];
  const u32 lds0 = (u32)(uintptr_t)(LDS_AS char*)smem;
  const u32 flags = lds0 + FLAGS;
  const int b = blockIdx.x, xcd = b & 7, jj = b >> 3;
  const int g = (xcd & 1) * 4 + (jj & 3), tg = (xcd >> 1) * 8 + (jj >> 2);
#define ROTEXPR ((((tg & 7) * 4 + (g & 3) * 8)) % 36)
  const int rot = ROTEXPR;
  const int w = __builtin_amdgcn_readfirstlane(threadIdx.x >> 6);
  const int l = threadIdx.x & 63;
  const rsrc_t rs = __builtin_amdgcn_make_buffer_rsrc((void*)ws, (short)0, (int)WS_END, 0x00020000);
  STAMP(1, w, 0);

  u32 ep;
  u32x4 xv[9];
  const u32 xo = (u32)((64 * tg + 8 * w + (l >> 3)) * 1152 + (l & 7) * 16);
#define XLD(i) asm volatile("global_load_dwordx4 %0, %1, %2 offset:" #i "*128" : "=v"(xv[i]) : "v"(xo), "s"(x) : "memory")
#define ENTRY_LOADS() do { u32 zero = 0; \
    asm volatile("global_load_dword %0, %1, %2 sc1" : "=v"(ep) : "v"(zero), "s"(ws) : "memory"); \
    XLD(0); XLD(1); XLD(2); XLD(3); XLD(4); XLD(5); XLD(6); XLD(7); XLD(8); } while (0)
  u32 wg[36];
  const u16* wsrc = g < 4 ? wk : wv;
  u32 o0, o1;
  {
    const int i = l & 15, kg = l >> 4, pi = i < 8 ? i : 8;
    o0 = (u32)((144 * (w & 3) + 4 * kg) * 36 + 4 * pi); o1 = o0 + 64 * 36;
  }
#define GLD(s, t) asm volatile("global_load_dword %0, %1, %2 offset:%3" : "=v"(wg[(s) * 4 + (t)]) : "v"((s) < 4 ? o0 : o1), "s"(wsrc), "n"((((s) < 4 ? (s) : (s) - 4) * 16 + (t)) * 36) : "memory")
#define GLD4(s) GLD(s, 0); GLD(s, 1); GLD(s, 2); GLD(s, 3)
  u32 wo9[9];
  auto wq_piece = [&](int j) {
    glds(lds0 + RING0 + (w + 4 * j) * 1024, wo9[j % 9], (const char*)wq + 144 * g + 4096 * (j % 9) + 294912 * (j / 9));
  };
  auto ln_body = [&](auto isF) {
    constexpr bool F = decltype(isF)::value;
    u32 pf = 0;
    ENTRY_LOADS();
    asm volatile("s_barrier" ::: "memory");
#define WQB(j0, n) do { if constexpr (F) { SCHED_FENCE(); _Pragma("unroll") for (int j = (j0); j < (j0) + (n) && j < 20; j++) wq_piece(j); SCHED_FENCE(); } } while (0)
    if constexpr (F) {
      const u32 n0 = 64 * w + l, mm = n0 * 7282u, b0 = n0 * 16u;
#pragma unroll
      for (int j = 0; j < 9; j++) wo9[j] = ((mm + (u32)j * (256u * 7282u)) >> 16) * 1008u + b0;
      asm volatile("" : "+v"(wo9[0]), "+v"(wo9[1]), "+v"(wo9[2]), "+v"(wo9[3]), "+v"(wo9[4]), "+v"(wo9[5]), "+v"(wo9[6]), "+v"(wo9[7]), "+v"(wo9[8]));
      WQB(0, WQ0);
      if (threadIdx.x < 32) lds_wr4(flags + 4 * threadIdx.x, 0u);
      waitvm<WQ0>();
    } else {
      waitvm<0>();
    }
    asm volatile("" : "+v"(xv[0]), "+v"(xv[1]), "+v"(xv[2]), "+v"(xv[3]), "+v"(xv[4]), "+v"(xv[5]), "+v"(xv[6]), "+v"(xv[7]), "+v"(xv[8]), "+v"(ep), "+v"(pf));
    STAMP(1, w, 1);
    u32 ones_u = 0x3F803F80u;
    asm volatile("" : "+v"(ones_u));
    float s0 = 0, s1 = 0, s2 = 0, s3 = 0;
    WQB(WQ0, WQA);
#pragma unroll
    for (int i = 0; i < 9; i++) {
      s0 = dot2bf(xv[i][0], ones_u, s0);
      s1 = dot2bf(xv[i][1], ones_u, s1);
      s2 = dot2bf(xv[i][2], ones_u, s2);
      s3 = dot2bf(xv[i][3], ones_u, s3);
    }
    if constexpr (F) asm volatile("" : "+v"(s0), "+v"(s1), "+v"(s2), "+v"(s3));
    WQB(WQ0 + WQA, WQB1);
    const float sum = red8((s0 + s1) + (s2 + s3));
    const float mean = sum * (1.0f / 576.0f);
    const float mb = rbf(mean);
    const f32x2 mb2 = {mb, mb};
    f32x2 qa = {0.f, 0.f}, qb = {0.f, 0.f};
    u32 sel_lo = 0x00003F80u, sel_hi = 0x3F800000u;
    asm volatile("" : "+v"(sel_lo), "+v"(sel_hi));
    const float nmb = -mb;
#pragma unroll
    for (int i = 0; i < 9; i++) {
      {
        float d[8];
        const u32 u0 = xv[i][0], u1 = xv[i][1], u2 = xv[i][2], u3 = xv[i][3];
        unpack_add8(u0, u1, u2, u3, sel_lo, sel_hi, nmb, d);
        const f32x2 a0 = {d[0], d[1]}, a1 = {d[2], d[3]}, a2 = {d[4], d[5]}, a3 = {d[6], d[7]};
        qa = __builtin_elementwise_fma(a0, a0, qa);
        qb = __builtin_elementwise_fma(a1, a1, qb);
        qa = __builtin_elementwise_fma(a2, a2, qa);
        qb = __builtin_elementwise_fma(a3, a3, qb);
        xv[i][0] = cvtpk(d[0], d[1]);
        xv[i][1] = cvtpk(d[2], d[3]);
        xv[i][2] = cvtpk(d[4], d[5]);
        xv[i][3] = cvtpk(d[6], d[7]);
      }
      if (i & 1) {
        if constexpr (F) asm volatile("" : "+v"(xv[i - 1]), "+v"(xv[i]), "+v"(qa), "+v"(qb));
        WQB(WQ0 + WQA + WQB1 + WQC * (i >> 1), WQC);
      }
    }
    if constexpr (F) asm volatile("" : "+v"(xv[8]), "+v"(qa), "+v"(qb));
    WQB(WQ0 + WQA + WQB1 + WQC * 4, 20);
    if constexpr (F) { if (w == 0) wq_piece(20); }
    qa += qb;
    const float sq = red8(qa[0] + qa[1]);
    const float dm = mean - mb;
    const float var = rbf(fmaxf(sq * (1.0f / 576.0f) - dm * dm, 0.0f));
    const float den = rbf(sqrtf(rbf(var + 1e-5f)));
    const float rden = 1.0f / den;
    const int kg = l & 7, tok32 = 8 * (w & 3) + (l >> 3);
    const u32 xw = lds0 + (kg >> 1) * 2048 + (w >> 2) * 1024 + (kg & 1) * 512 + ((tok32 ^ kg) * 16);
#pragma unroll
    for (int i = 0; i < 9; i++) {
      u32x4 o;
#pragma unroll
      for (int k = 0; k < 4; k++) {
        const u32 u0 = xv[i][k];
        const f32x2 a = f32x2{lo2f(u0), hi2f(u0)} * f32x2{rden, rden};
        o[k] = cvtpk(a[0], a[1]);
      }
      lds_wr16(xw + i * 8192, o);
    }
#undef WQB
  };
  if (w < 4) ln_body(std::true_type{}); else ln_body(std::false_type{});
  const u32 tag = __builtin_amdgcn_readfirstlane(ep) + 1u;
  if (w >= 4) { GLD4(0); GLD4(1); GLD4(2); GLD4(3); GLD4(4); GLD4(5); GLD4(6); GLD4(7); GLD4(8); }
  STAMP(1, w, 2);
  if (w < 4) waitvm<0>();
  rawbar();

  if (w < 4) {
    __builtin_amdgcn_s_setprio(PRIO_Q);
    ffn_body(wo, w1, w3, w2, ws, rs, lds0, w, g, tg, rot, tag);
  } else {
    const int aw = w - 4;
    if (PRIO_KV != 0) __builtin_amdgcn_s_setprio(PRIO_KV);
    waitvm<0>();
    {
      asm volatile("" : "+v"(wg[0]), "+v"(wg[1]), "+v"(wg[2]), "+v"(wg[3]), "+v"(wg[4]), "+v"(wg[5]), "+v"(wg[6]), "+v"(wg[7]), "+v"(wg[8]),
                   "+v"(wg[9]), "+v"(wg[10]), "+v"(wg[11]), "+v"(wg[12]), "+v"(wg[13]), "+v"(wg[14]), "+v"(wg[15]), "+v"(wg[16]), "+v"(wg[17]));
      asm volatile("" : "+v"(wg[18]), "+v"(wg[19]), "+v"(wg[20]), "+v"(wg[21]), "+v"(wg[22]), "+v"(wg[23]), "+v"(wg[24]), "+v"(wg[25]), "+v"(wg[26]),
                   "+v"(wg[27]), "+v"(wg[28]), "+v"(wg[29]), "+v"(wg[30]), "+v"(wg[31]), "+v"(wg[32]), "+v"(wg[33]), "+v"(wg[34]), "+v"(wg[35]));
      const int T16 = g & 3, i = l & 15, kg = l >> 4, hh = kg >> 1;
      const int tok32 = 16 * (T16 & 1) + i;
      const u32 xb = lds0 + (T16 >> 1) * 1024 + hh * 512 + ((tok32 ^ hh) * 16) + (kg & 1) * 8;
      f32x4 E = {0, 0, 0, 0}, O = {0, 0, 0, 0};
      const bool isK = g < 4;
#pragma unroll
      for (int s = 0; s < 9; s++) {
        const int ks = 9 * aw + s;
        const u32x2 xo = lds_rd8((xb ^ (32 * (ks & 3))) + ks * 2048);
        const u32x2 we = {__builtin_amdgcn_perm(wg[4 * s + 1], wg[4 * s], 0x05040100u), __builtin_amdgcn_perm(wg[4 * s + 3], wg[4 * s + 2], 0x05040100u)};
        const u32x2 wd = {__builtin_amdgcn_perm(wg[4 * s + 1], wg[4 * s], 0x07060302u), __builtin_amdgcn_perm(wg[4 * s + 3], wg[4 * s + 2], 0x07060302u)};
        if (isK) { E = mfma16k16(we, xo, E); O = mfma16k16(wd, xo, O); }
        else { E = mfma16k16(xo, we, E); O = mfma16k16(xo, wd, O); }
      }
      if (aw != 0) {
        lds_wr16(lds0 + KVRED + ((aw - 1) * 2 + 0) * 1024 + l * 16, __builtin_bit_cast(u32x4, E));
        lds_wr16(lds0 + KVRED + ((aw - 1) * 2 + 1) * 1024 + l * 16, __builtin_bit_cast(u32x4, O));
        lds_flag_set(flags + F_KVP + 4 * aw, 1u);
      } else {
        lds_flag_set(flags + F_KVP, 1u);
        lds_flag_wait4(flags + F_KVP, 1u);
#pragma unroll
        for (int j = 0; j < 3; j++) {
          E += __builtin_bit_cast(f32x4, lds_rd16(lds0 + KVRED + (j * 2 + 0) * 1024 + l * 16));
          O += __builtin_bit_cast(f32x4, lds_rd16(lds0 + KVRED + (j * 2 + 1) * 1024 + l * 16));
        }
        const int t16 = 4 * tg + T16, rg = kg;
        if (isK) {
          const u32x4 kv = {cvtpk(E[0], O[0]), cvtpk(E[1], O[1]), cvtpk(E[2], O[2]), cvtpk(E[3], O[3])};
          if (rg < 2) st16c(rs, K1F_OFF + (rg * 32 + 16 * (t16 & 1) + i) * 16, (u32)(t16 >> 1) * 1024u, kv);
          else if (rg == 2) st4c(rs, K2_OFF + i * 4, (u32)t16 * 64u, kv[0]);
        } else {
          const u32 vb = VF_OFF + (rg & 1) * 512 + (rg >> 1) * 8;
          u32x2 e = {cvtpk(E[0], E[1]), cvtpk(E[2], E[3])}, o = {cvtpk(O[0], O[1]), cvtpk(O[2], O[3])};
          if (i == 9) e = u32x2{0x3F803F80u, 0x3F803F80u};
          if (i <= 9) st8c(rs, vb + 2 * i * 16, (u32)t16 * 1024u, e);
          if (i <= 8) st8c(rs, vb + (2 * i + 1) * 16, (u32)t16 * 1024u, o);
        }
        waitvm<0>();
        if (l < KVREP) st4c(rs, KVF_OFF + l * 1024 + b * 4, 0, tag);
      }
    }
    STAMP(1, w, 3);
    u32x4 Q1[4], Q2[4];
    const u32 kfo = KVF_OFF + (b % KVREP) * 1024 + l * 16;
    {
      lds_flag_wait4(flags + F_QRD, 1u);
      const u32 qb = lds0 + QSLOT + (aw >> 1) * 1152;
#pragma unroll
      for (int i = 0; i < 4; i++) {
        Q1[i] = lds_rd16(qb + i * STG + l * 16);
        const u32 q2 = lds_rd4(qb + i * STG + 1024 + (l & 31) * 4);
        Q2[i] = u32x4{(l >> 5) ? 0u : q2, 0u, 0u, 0u};
      }
      asm volatile("" : "+v"(Q1[0]), "+v"(Q1[1]), "+v"(Q1[2]), "+v"(Q1[3]), "+v"(Q2[0]), "+v"(Q2[1]), "+v"(Q2[2]), "+v"(Q2[3]));
      lds_flag_set(flags + F_QCN + 4 * aw, 1u);
    }
    STAMP(1, w, 4);
    const bool kvall = false;
    if (aw == 0) {
      if (!kvall) poll16(ws, kfo, tag);
      lds_flag_set(flags + F_KVD, 1u);
      if (b == 0 && l == 0) st4c(rs, EPOCH_OFF, 0, tag);
    } else if (!kvall) {
      lds_flag_wait1(flags + F_KVD, 1u);
    }
    STAMP(1, w, 5);
    __builtin_amdgcn_s_setprio(PRIO_ATT);
    attn_body<4>(ws, rs, lds0, aw, g, tg, tag, Q1, Q2);
    __builtin_amdgcn_s_setprio(0);
  }

  {
    const int tt = w >> 2, kq = w & 3;
    const int h = l >> 5, c31 = l & 31;
    const bool hasx = tt == (g >> 2);
    const int token = 64 * tg + 32 * tt + c31;
    const int col = 32 * (g + 8 * (kq >> 1)) + 16 * (kq & 1) + 4 * h;
    const int colx = 512 + 16 * (g & 3) + 4 * h;
    const u32x2 xr0 = *(const u32x2*)(x + (size_t)token * 576 + col), xr1 = *(const u32x2*)(x + (size_t)token * 576 + col + 8);
    u32x2 xx0 = {0, 0}, xx1 = {0, 0};
    if (hasx && kq == 0) { xx0 = *(const u32x2*)(x + (size_t)token * 576 + colx); xx1 = *(const u32x2*)(x + (size_t)token * 576 + colx + 8); }
    rawbar();
    STAMP(1, w, 13);
    u32x4 of[9];
#pragma unroll
    for (int i = 0; i < 8; i++) of[i] = ld16c(rs, l * 16, OF_OFF + (u32)((tg * 36 + 8 * kq + i) * 2 + tt) * 1024u);
    of[8] = ld16c(rs, l * 16, OF_OFF + (u32)((tg * 36 + 32 + kq) * 2 + tt) * 1024u);
    const int G1 = (l >> 4) & 1, q4 = (l & 15) >> 2, p4 = l & 3;
    const u32 cs = (u32)(G1 * 32 + 8 * p4) ^ (u32)(((q4 >> 1) & 1) * 64);
    const u32 aE = lds0 + (8 * kq * 18 + 4 * h + q4) * 128 + cs;
    const int rowx = 18 * (8 * kq + 4 * h + (q4 >> 1)) + 16 + (q4 & 1);
    const u32 aX = lds0 + rowx * 128 + cs;
    const u32 eE = lds0 + WOX0 + (8 * kq * 18 + 4 * h + q4) * 32 + 8 * p4;
    const u32 eX = lds0 + WOX0 + rowx * 32 + 8 * p4;
    f32x16 sa[2] = {f32x16{}, f32x16{}}, sx = {};
#pragma unroll
    for (int i = 0; i < 8; i++) {
#pragma unroll
      for (int d = 0; d < 2; d++) {
        const u32 a = (aE ^ (64 * ((i + d) & 1))) + i * 2304;
        sa[d] = mfma32(tr2(a, a + 1024), of[i], sa[d]);
      }
      if (hasx) sx = mfma32(tr2(eE + i * 576, eE + i * 576 + 256), of[i], sx);
    }
#pragma unroll
    for (int d = 0; d < 2; d++) {
      const u32 a = aX ^ (64 * d);
      sa[d] = mfma32(tr2(a, a + 36 * 128), of[8], sa[d]);
    }
    if (hasx) sx = mfma32(tr2(eX, eX + 36 * 32), of[8], sx);
    const u32 rb = lds0 + RED0 + l * 16;
#pragma unroll
    for (int pi = 0; pi < 4; pi++) {
      if (pi != kq) {
        const f32x16& s = sa[pi >> 1];
        const int o = 8 * (pi & 1);
        const u32 a = rb + (u32)((tt * 5 + pi) * 3 + ((kq - pi - 1) & 3)) * 2048u;
        lds_wr16(a, __builtin_bit_cast(u32x4, f32x4{s[o], s[o + 1], s[o + 2], s[o + 3]}));
        lds_wr16(a + 1024, __builtin_bit_cast(u32x4, f32x4{s[o + 4], s[o + 5], s[o + 6], s[o + 7]}));
      }
    }
    if (hasx && kq != 0) {
      const u32 a = rb + (u32)((tt * 5 + 4) * 3 + (kq - 1)) * 2048u;
      lds_wr16(a, __builtin_bit_cast(u32x4, f32x4{sx[0], sx[1], sx[2], sx[3]}));
      lds_wr16(a + 1024, __builtin_bit_cast(u32x4, f32x4{sx[4], sx[5], sx[6], sx[7]}));
    }
    rawbar();
    auto finish = [&](float (&s)[8], int pi, int gitem, u32x2 xa, u32x2 xb2, int c) {
#pragma unroll
      for (int j = 0; j < 3; j++) {
        const u32 a = rb + (u32)((tt * 5 + pi) * 3 + j) * 2048u;
        const f32x4 p0 = __builtin_bit_cast(f32x4, lds_rd16(a)), p1 = __builtin_bit_cast(f32x4, lds_rd16(a + 1024));
#pragma unroll
        for (int r = 0; r < 4; r++) { s[r] += p0[r]; s[4 + r] += p1[r]; }
      }
      const u32x4 gv = lds_rd16(lds0 + GG0 + gitem * 1024 + l * 16);
      const u32 g0 = gv[0], g1 = gv[1], g2 = gv[2], g3 = gv[3];
      const u32 x0 = xa[0], x1 = xa[1], x2 = xb2[0], x3 = xb2[1];
      u32x2 o0, o1;
      o0[0] = cvtpk(lo2f(x0) + rbf(rbf(s[0]) * lo2f(g0)), hi2f(x0) + rbf(rbf(s[1]) * hi2f(g0)));
      o0[1] = cvtpk(lo2f(x1) + rbf(rbf(s[2]) * lo2f(g1)), hi2f(x1) + rbf(rbf(s[3]) * hi2f(g1)));
      o1[0] = cvtpk(lo2f(x2) + rbf(rbf(s[4]) * lo2f(g2)), hi2f(x2) + rbf(rbf(s[5]) * hi2f(g2)));
      o1[1] = cvtpk(lo2f(x3) + rbf(rbf(s[6]) * lo2f(g3)), hi2f(x3) + rbf(rbf(s[7]) * hi2f(g3)));
      *(u32x2*)(out + (size_t)token * 576 + c) = o0;
      *(u32x2*)(out + (size_t)token * 576 + c + 8) = o1;
    };
    {
      float s[8];
#pragma unroll
      for (int r = 0; r < 8; r++) {
        const float v0 = (kq & 1) ? sa[0][8 + r] : sa[0][r], v1 = (kq & 1) ? sa[1][8 + r] : sa[1][r];
        s[r] = (kq >> 1) ? v1 : v0;
      }
      finish(s, kq, tt * 4 + kq, xr0, xr1, col);
    }
    if (hasx && kq == 0) {
      float s[8];
#pragma unroll
      for (int r = 0; r < 8; r++) s[r] = sx[r];
      finish(s, 4, 8, xx0, xx1, colx);
    }
    STAMP(1, w, 14);
  }
}

extern "C" void kernel_launch(void* const* d_in, const int* in_sizes, int n_in, void* d_out, int out_size, void* d_ws,
                              size_t ws_size, hipStream_t stream) {
  const u16* x = (const u16*)d_in[0];
  const u16* wq = (const u16*)d_in[1];
  const u16* wk = (const u16*)d_in[2];
  const u16* wv = (const u16*)d_in[3];
  const u16* wo = (const u16*)d_in[4];
  const u16* w1 = (const u16*)d_in[6];
  const u16* w3 = (const u16*)d_in[7];
  const u16* w2 = (const u16*)d_in[8];
  char* ws = (char*)d_ws;
  (void)hipFuncSetAttribute((const void*)fused_kernel, hipFuncAttributeMaxDynamicSharedMemorySize, LDS_TOTAL);
  fused_kernel<<<256, 512, LDS_TOTAL, stream>>>(x, wq, wk, wv, wo, w1, w3, w2, ws, (u16*)d_out);
}
```
